```python
import torch

def _softmax(x, axis=-1):
    return torch.softmax(x, dim=axis)
def _gelu(x):
    return torch.nn.functional.gelu(x, approximate="tanh")
def _silu(x):
    return torch.nn.functional.silu(x)
def _sigmoid(x):
    return torch.sigmoid(x)
def _layernorm(x, axis=-1, eps=1e-5):
    m = x.mean(dim=axis, keepdim=True)
    v = x.var(dim=axis, keepdim=True, unbiased=False)
    return (x - m) / torch.sqrt(v + eps)
def _scatter_add(tgt, idx, src, axis=0):
    return tgt.clone().index_add_(axis, idx, src)
def _segment_sum(src, seg, n):
    out = torch.zeros((n,) + src.shape[1:], dtype=src.dtype)
    return out.index_add_(0, seg, src)
def _pad(x, pw):
    flat = tuple(v for pair in reversed(pw) for v in pair)
    return torch.nn.functional.pad(x, flat)

kernel_name = 'concat_heads__mha_block_cached__gqa_block_cached__near_ridge'

def setup_inputs():

    _w = (lambda a: (lambda t: t.to(torch.bfloat16) if t.is_floating_point() else t)(torch.as_tensor(a)))
    def _A_setup():
        B, Sq, D = 32, 48, 448
        H, Sk, Dh = 16, 48, 256
        return {
            "x": _w((lambda *s: torch.randn(*s, dtype=torch.bfloat16) * 0.8)(B, Sq, D)),
            "k_cache": _w((lambda *s: torch.randn(*s, dtype=torch.bfloat16) * 0.8)(B, H, Sk, Dh)),
            "v_cache": _w((lambda *s: torch.randn(*s, dtype=torch.bfloat16) * 0.05)(B, H, Sk, Dh)),
            "wq": _w((lambda *s: torch.randn(*s, dtype=torch.bfloat16) * 0.8)(D, H * Dh) * (D ** -0.5)),
            "wo": _w((lambda *s: torch.randn(*s, dtype=torch.bfloat16) * 0.05)(H * Dh, D) * ((H * Dh) ** -0.5)),
        }
    _A_in = _A_setup()

    def _B_setup():
        B, Sq, D = 32, 48, 448
        Hq, Hkv, Sk, Dh = 16, 16, 48, 256
        assert Hq % Hkv == 0, (Hq, Hkv)
        return {
            "x": _w((lambda *s: torch.randn(*s, dtype=torch.bfloat16) * 0.8)(B, Sq, D)),
            "k_cache": _w((lambda *s: torch.randn(*s, dtype=torch.bfloat16) * 0.8)(B, Hkv, Sk, Dh)),
            "v_cache": _w((lambda *s: torch.randn(*s, dtype=torch.bfloat16) * 0.05)(B, Hkv, Sk, Dh)),
            "wq": _w((lambda *s: torch.randn(*s, dtype=torch.bfloat16) * 0.8)(D, Hq * Dh) * (D ** -0.5)),
            "wo": _w((lambda *s: torch.randn(*s, dtype=torch.bfloat16) * 0.05)(Hq * Dh, D) * ((Hq * Dh) ** -0.5)),
        }
    _B_in = _B_setup()

    return {
        "A_x": _A_in["x"],
        "A_k_cache": _A_in["k_cache"],
        "A_v_cache": _A_in["v_cache"],
        "A_wq": _A_in["wq"],
        "A_wo": _A_in["wo"],
        "B_x": _B_in["x"],
        "B_k_cache": _B_in["k_cache"],
        "B_v_cache": _B_in["v_cache"],
        "B_wq": _B_in["wq"],
        "B_wo": _B_in["wo"],
    }

def reference(A_x, A_k_cache, A_v_cache, A_wq, A_wo, B_x, B_k_cache, B_v_cache, B_wq, B_wo):
    def _A_ref(x, k_cache, v_cache, wq, wo):
        B, Sq, D = x.shape
        H, Dh = 16, 256
        q = (lambda x, *d: x.permute(*d))((x @ wq).reshape(B, Sq, H, Dh), 0, 2, 1, 3)
        scores = torch.einsum("bhqd,bhkd->bhqk", q, k_cache) * (Dh ** -0.5)
        p = _softmax(scores, axis=-1)
        o = torch.einsum("bhqk,bhkd->bhqd", p, v_cache)
        return (lambda x, *d: x.permute(*d))(o, 0, 2, 1, 3).reshape(B, Sq, H * Dh) @ wo

    def _B_ref(x, k_cache, v_cache, wq, wo):
        B, Sq, D = x.shape
        Hq, Dh = 16, 256
        Hkv = k_cache.shape[1]
        rep = Hq // Hkv
        q = (lambda x, *d: x.permute(*d))((x @ wq).reshape(B, Sq, Hq, Dh), 0, 2, 1, 3)
        k = (lambda x, n, axis: torch.repeat_interleave(x, n, dim=axis))(k_cache, rep, axis=1)
        v = (lambda x, n, axis: torch.repeat_interleave(x, n, dim=axis))(v_cache, rep, axis=1)
        scores = torch.einsum("bhqd,bhkd->bhqk", q, k) * (Dh ** -0.5)
        p = _softmax(scores, axis=-1)
        o = torch.einsum("bhqk,bhkd->bhqd", p, v)
        return (lambda x, *d: x.permute(*d))(o, 0, 2, 1, 3).reshape(B, Sq, Hq * Dh) @ wo

    a = _A_ref(A_x, A_k_cache, A_v_cache, A_wq, A_wo)
    b = _B_ref(B_x, B_k_cache, B_v_cache, B_wq, B_wo)
    return torch.concatenate([a, b], axis=-1)
```

```cpp
#include <hip/hip_runtime.h>
#include <hip/hip_bf16.h>
typedef unsigned short u16;
__device__ __forceinline__ float bf2f(u16 b){ return __uint_as_float(((unsigned)b)<<16); }
__device__ __forceinline__ u16 f2bf(float f){ unsigned u=__float_as_uint(f); u += 0x7fffu + ((u>>16)&1u); return (u16)(u>>16); }
__global__ __launch_bounds__(256) void ngemm(const u16* A, const u16* B, u16* C, int M, int N, int K, int ldc){
  int n = blockIdx.x*64 + (threadIdx.x&63); int m = blockIdx.y*4 + (threadIdx.x>>6);
  if(n>=N||m>=M) return;
  float acc=0.f;
  for(int k=0;k<K;++k) acc += bf2f(A[(size_t)m*K+k])*bf2f(B[(size_t)k*N+n]);
  C[(size_t)m*ldc+n]=f2bf(acc);
}
__global__ __launch_bounds__(256) void nattn(const u16* q, const u16* kc, const u16* vc, u16* o){
  int b=blockIdx.x>>4, h=blockIdx.x&15;
  __shared__ float s[48][48];
  const u16* K=kc+(size_t)(b*16+h)*48*256; const u16* V=vc+(size_t)(b*16+h)*48*256;
  for(int i=threadIdx.x;i<48*48;i+=256){ int r=i/48,c=i%48; float a=0.f;
    for(int d=0;d<256;++d) a+=bf2f(q[(size_t)(b*48+r)*4096+h*256+d])*bf2f(K[c*256+d]);
    s[r][c]=bf2f(f2bf(a))*0.0625f; }
  __syncthreads();
  if(threadIdx.x<48){ int r=threadIdx.x; float m=-1e30f; for(int c=0;c<48;++c) m=fmaxf(m,s[r][c]);
    float z=0.f; for(int c=0;c<48;++c){ float e=__expf(s[r][c]-m); s[r][c]=e; z+=e; }
    for(int c=0;c<48;++c) s[r][c]=bf2f(f2bf(s[r][c]/z)); }
  __syncthreads();
  int d=threadIdx.x;
  for(int r=0;r<48;++r){ float a=0.f; for(int c=0;c<48;++c) a+=s[r][c]*bf2f(V[c*256+d]); o[(size_t)(b*48+r)*4096+h*256+d]=f2bf(a); }
}
extern "C" void kernel_launch(void* const* d_in, const int* in_sizes, int n_in, void* d_out, int out_size, void* d_ws, size_t ws_size, hipStream_t stream){
  u16* q=(u16*)d_ws; u16* o=q+(size_t)1536*4096;
  for(int br=0;br<2;++br){
    const u16* x=(const u16*)d_in[br*5+0]; const u16* k=(const u16*)d_in[br*5+1]; const u16* v=(const u16*)d_in[br*5+2];
    const u16* wq=(const u16*)d_in[br*5+3]; const u16* wo=(const u16*)d_in[br*5+4];
    ngemm<<<dim3(64,384),256,0,stream>>>(x,wq,q,1536,4096,448,4096);
    nattn<<<512,256,0,stream>>>(q,k,v,o);
    ngemm<<<dim3(7,384),256,0,stream>>>(o,wo,(u16*)d_out+br*448,1536,448,4096,896);
  }
}
```

```cpp
#include <hip/hip_runtime.h>
#include <stdint.h>

__global__ __launch_bounds__(512, 2) void fused_attn_pair(
    const char* ax, const char* ak, const char* av, const char* awq, const char* awo,
    const char* bx, const char* bk, const char* bv, const char* bwq, const char* bwo, char* out, char* ws) {
  __shared__ __attribute__((aligned(256))) char smem[163840];
  const unsigned wave = __builtin_amdgcn_readfirstlane(threadIdx.x >> 6);
  asm volatile(
    "s_bfe_u32 s31, %1, 0x10002\n"
    "s_mul_i32 s36, s31, 40\n"
    "s_load_dwordx8 s[12:19], %2, s36\n"
    "s_add_u32 s36, s36, 32\n"
    "s_load_dwordx2 s[20:21], %2, s36\n"
    "s_load_dwordx4 s[24:27], %2, 0x50\n"
    "v_mbcnt_lo_u32_b32 v255, -1, 0\n"
    "v_mbcnt_hi_u32_b32 v255, -1, v255\n"
    "s_getpc_b64 s[36:37]\n"
    "L_pc0:\n"
    "s_and_b32 s38, %1, 0xf8\n"
    "s_add_u32 s38, s38, %0\n"
    "s_mul_i32 s38, s38, 256\n"
    "v_lshl_add_u32 v254, v255, 7, s38\n"
    "v_min_u32 v254, L_end-L_pc0, v254\n"
    "s_mov_b64 exec, 3\n"
    "global_load_dword v253, v254, s[36:37]\n"
    "s_mov_b64 exec, -1\n"
    "s_and_b32 s28, %1, 3\n"
    "s_bfe_u32 s29, %1, 0x20003\n"
    "s_lshr_b32 s30, %1, 5\n"
    "s_add_u32 s35, s30, s29\n"
    "s_and_b32 s9, s35, 1\n"
    "s_mul_i32 s9, s9, 3\n"
    "s_and_b32 s35, s35, 3\n"
    "s_and_b32 s33, s30, 1\n"
    "s_cmp_lt_u32 %0, 4\n"
    "s_cbranch_scc1 L_cons\n"
    "L_prod0:\n"
    "s_sub_u32 s56, %0, 4\n"
    "v_lshrrev_b32 v243, 3, v255\n"
    "v_and_b32 v244, 7, v255\n"
    "v_and_b32 v245, 6, v243\n"
    "v_xor_b32 v244, v244, v245\n"
    "v_lshlrev_b32 v244, 4, v244\n"
    "s_mul_i32 s57, s56, 43008\n"
    "v_mul_u32_u24 v245, 896, v243\n"
    "v_add3_u32 v216, v245, v244, s57\n"
    "v_add_u32 v217, 6144, v216\n"
    "v_add_u32 v218, 12288, v216\n"
    "v_add_u32 v219, 21504, v216\n"
    "v_add_u32 v220, 27648, v216\n"
    "v_add_u32 v221, 33792, v216\n"
    "s_mul_i32 s59, s56, 24576\n"
    "s_add_u32 s59, s59, 65536\n"
    "s_mov_b32 s70, s9\n"
    "s_lshl_b32 s58, s56, 12\n"
    "v_lshrrev_b32 v246, 5, v255\n"
    "v_and_b32 v247, 31, v255\n"
    "v_lshlrev_b32 v245, 1, v246\n"
    "v_xor_b32 v247, v247, v245\n"
    "s_and_b32 s57, s56, 1\n"
    "s_lshl_b32 s57, s57, 2\n"
    "s_lshr_b32 s61, s56, 1\n"
    "s_add_u32 s57, s57, s61\n"
    "s_lshl_b32 s57, s57, 15\n"
    "v_lshl_add_u32 v246, v246, 13, s57\n"
    "s_lshl_b32 s57, s35, 3\n"
    "v_xor_b32 v245, 0, v247\n"
    "v_add_u32 v245, s57, v245\n"
    "v_and_b32 v245, 31, v245\n"
    "v_lshl_add_u32 v212, v245, 4, v246\n"
    "v_xor_b32 v245, 4, v247\n"
    "v_add_u32 v245, s57, v245\n"
    "v_and_b32 v245, 31, v245\n"
    "v_lshl_add_u32 v213, v245, 4, v246\n"
    "v_add_u32 v213, 15360, v213\n"
    "v_xor_b32 v245, 8, v247\n"
    "v_add_u32 v245, s57, v245\n"
    "v_and_b32 v245, 31, v245\n"
    "v_lshl_add_u32 v214, v245, 4, v246\n"
    "v_add_u32 v214, 63488, v214\n"
    "v_xor_b32 v245, 12, v247\n"
    "v_add_u32 v245, s57, v245\n"
    "v_and_b32 v245, 31, v245\n"
    "v_lshl_add_u32 v215, v245, 4, v246\n"
    "v_add_u32 v215, 78848, v215\n"
    "s_mul_i32 s57, s30, 172032\n"
    "s_mov_b32 s42, 172032\n"
    "s_mov_b32 s43, 0x00020000\n"
    "s_lshl_b32 s61, s28, 2\n"
    "s_add_u32 s61, s61, s29\n"
    "s_lshl_b32 s63, s61, 9\n"
    "s_mov_b32 s38, 3662336\n"
    "s_mov_b32 s39, 0x00020000\n"
    "s_waitcnt lgkmcnt(0)\n"
    "s_add_u32 s40, s12, s57\n"
    "s_addc_u32 s41, s13, 0\n"
    "s_add_u32 s36, s18, s63\n"
    "s_addc_u32 s37, s19, 0\n"
    "s_add_u32 m0, s59, 0\n"
    "s_lshl_b32 s62, s70, 7\n"
    "buffer_load_dwordx4 v216, s[40:43], s62 offen offset:0 lds\n"
    "buffer_load_dwordx4 v217, s[40:43], s62 offen offset:1024 lds\n"
    "buffer_load_dwordx4 v218, s[40:43], s62 offen offset:2048 lds\n"
    "s_add_u32 m0, s59, 3072\n"
    "s_nop 0\n"
    "buffer_load_dwordx4 v219, s[40:43], s62 offen offset:0 lds\n"
    "buffer_load_dwordx4 v220, s[40:43], s62 offen offset:1024 lds\n"
    "buffer_load_dwordx4 v221, s[40:43], s62 offen offset:2048 lds\n"
    "s_add_u32 m0, s58, 0\n"
    "s_lshl_b32 s62, s70, 19\n"
    "buffer_load_dwordx4 v212, s[36:39], s62 offen offset:0 lds\n"
    "buffer_load_dwordx4 v213, s[36:39], s62 offen offset:1024 lds\n"
    "buffer_load_dwordx4 v214, s[36:39], s62 offen offset:2048 lds\n"
    "buffer_load_dwordx4 v215, s[36:39], s62 offen offset:3072 lds\n"
    "s_add_u32 s71, s9, 1\n"
    "s_cmp_ge_u32 s71, 7\n"
    "s_cselect_b32 s61, 7, 0\n"
    "s_sub_u32 s71, s71, s61\n"
    "s_add_u32 s72, s9, 2\n"
    "s_cmp_ge_u32 s72, 7\n"
    "s_cselect_b32 s61, 7, 0\n"
    "s_sub_u32 s72, s72, s61\n"
    "s_add_u32 s73, s9, 3\n"
    "s_cmp_ge_u32 s73, 7\n"
    "s_cselect_b32 s61, 7, 0\n"
    "s_sub_u32 s73, s73, s61\n"
    "s_add_u32 s74, s9, 4\n"
    "s_cmp_ge_u32 s74, 7\n"
    "s_cselect_b32 s61, 7, 0\n"
    "s_sub_u32 s74, s74, s61\n"
    "s_add_u32 s75, s9, 5\n"
    "s_cmp_ge_u32 s75, 7\n"
    "s_cselect_b32 s61, 7, 0\n"
    "s_sub_u32 s75, s75, s61\n"
    "s_add_u32 s76, s9, 6\n"
    "s_cmp_ge_u32 s76, 7\n"
    "s_cselect_b32 s61, 7, 0\n"
    "s_sub_u32 s76, s76, s61\n"
    "s_add_u32 m0, s58, 16384\n"
    "s_lshl_b32 s62, s70, 19\n"
    "s_add_u32 s62, s62, 262144\n"
    "buffer_load_dwordx4 v212, s[36:39], s62 offen offset:0 lds\n"
    "buffer_load_dwordx4 v213, s[36:39], s62 offen offset:1024 lds\n"
    "buffer_load_dwordx4 v214, s[36:39], s62 offen offset:2048 lds\n"
    "buffer_load_dwordx4 v215, s[36:39], s62 offen offset:3072 lds\n"
    "s_add_u32 m0, s58, 32768\n"
    "s_lshl_b32 s62, s71, 19\n"
    "buffer_load_dwordx4 v212, s[36:39], s62 offen offset:0 lds\n"
    "buffer_load_dwordx4 v213, s[36:39], s62 offen offset:1024 lds\n"
    "buffer_load_dwordx4 v214, s[36:39], s62 offen offset:2048 lds\n"
    "buffer_load_dwordx4 v215, s[36:39], s62 offen offset:3072 lds\n"
    "s_lshl_b32 s54, s31, 3\n"
    "s_add_u32 s54, s54, s30\n"
    "s_lshl_b32 s55, s54, 7\n"
    "s_load_dwordx2 s[52:53], s[26:27], s55\n"
    "s_and_b32 s22, s30, 1\n"
    "s_lshr_b32 s23, s30, 1\n"
    "s_add_u32 s23, s23, s29\n"
    "s_and_b32 s23, s23, 3\n"
    "s_mov_b32 s34, 0x1065432\n"
    "s_and_b32 s57, s29, 1\n"
    "s_lshl_b32 s57, s57, 2\n"
    "s_add_u32 s57, s57, s28\n"
    "s_cmp_eq_u32 s57, 1\n"
    "s_cselect_b32 s34, 0x3210654, s34\n"
    "s_cmp_eq_u32 s57, 2\n"
    "s_cselect_b32 s34, 0x5432106, s34\n"
    "s_cmp_eq_u32 s57, 3\n"
    "s_cselect_b32 s34, 0x6543210, s34\n"
    "s_cmp_eq_u32 s57, 4\n"
    "s_cselect_b32 s34, 0x2106543, s34\n"
    "s_cmp_eq_u32 s57, 5\n"
    "s_cselect_b32 s34, 0x4321065, s34\n"
    "s_cmp_eq_u32 s57, 6\n"
    "s_cselect_b32 s34, 0x6543210, s34\n"
    "s_cmp_eq_u32 s57, 7\n"
    "s_cselect_b32 s34, 0x0654321, s34\n"
    "s_lshl_b32 s57, s30, 4\n"
    "s_add_u32 s57, s57, s28\n"
    "s_lshl_b32 s57, s57, 2\n"
    "s_mul_i32 s61, s57, 24576\n"
    "s_add_u32 s16, s16, s61\n"
    "s_addc_u32 s17, s17, 0\n"
    "s_add_u32 s57, s57, s29\n"
    "s_mul_i32 s61, s57, 24576\n"
    "s_add_u32 s14, s14, s61\n"
    "s_addc_u32 s15, s15, 0\n"
    "s_mul_i32 s57, s28, 917504\n"
    "s_mul_i32 s61, s29, 224\n"
    "s_add_u32 s57, s57, s61\n"
    "s_add_u32 s20, s20, s57\n"
    "s_addc_u32 s21, s21, 0\n"
    "s_mov_b64 s[44:45], s[16:17]\n"
    "s_mov_b32 s46, 1277952\n"
    "s_mov_b32 s47, 0x00020000\n"
    "s_mov_b64 s[48:49], s[20:21]\n"
    "s_mov_b32 s50, 916832\n"
    "s_mov_b32 s51, 0x00020000\n"
    "s_mov_b64 s[88:89], s[14:15]\n"
    "s_mov_b32 s90, 1204224\n"
    "s_mov_b32 s91, 0x00020000\n"
    "s_mov_b64 s[92:93], s[26:27]\n"
    "s_mov_b32 s94, 20512768\n"
    "s_mov_b32 s95, 0x00020000\n"
    "s_mul_i32 s57, s56, 393216\n"
    "v_lshl_add_u32 v222, v243, 9, v244\n"
    "v_add_u32 v222, s57, v222\n"
    "v_lshl_add_u32 v233, v255, 9, s57\n"
    "v_add_u32 v223, 3072, v222\n"
    "v_add_u32 v224, 6144, v222\n"
    "v_add_u32 v225, 12288, v222\n"
    "v_add_u32 v226, 15360, v222\n"
    "v_add_u32 v227, 18432, v222\n"
    "v_mul_u32_u24 v246, 4682, v255\n"
    "v_lshrrev_b32 v246, 16, v246\n"
    "v_mul_u32_u24 v247, 14, v246\n"
    "v_sub_u32 v247, v255, v247\n"
    "v_and_b32 v245, 14, v247\n"
    "v_lshlrev_b32 v245, 1, v245\n"
    "v_lshrrev_b32 v245, v245, s34\n"
    "v_and_b32 v245, 7, v245\n"
    "v_and_b32 v247, 1, v247\n"
    "v_lshl_add_u32 v247, v245, 1, v247\n"
    "v_lshlrev_b32 v247, 4, v247\n"
    "v_mul_u32_u24 v246, 896, v246\n"
    "s_lshr_b32 s57, s56, 1\n"
    "s_lshl_b32 s57, s57, 3\n"
    "s_and_b32 s61, s56, 1\n"
    "s_add_u32 s57, s57, s61\n"
    "s_mul_i32 s57, s57, 3584\n"
    "v_add3_u32 v228, v246, v247, s57\n"
    "v_add_u32 v229, 6272, v228\n"
    "v_add_u32 v230, 12544, v228\n"
    "v_add_u32 v231, 18816, v228\n"
    "s_mul_i32 s60, s56, 3584\n"
    "v_lshlrev_b32 v240, 3, v255\n"
    "v_lshlrev_b32 v232, 4, v255\n"
    "v_add_u32 v234, s60, v232\n"
    "v_add_u32 v235, s59, v232\n"
    "s_add_u32 s80, s22, 0\n"
    "s_and_b32 s80, s80, 3\n"
    "s_add_u32 s84, s23, 0\n"
    "s_and_b32 s84, s84, 3\n"
    "s_add_u32 s81, s22, 1\n"
    "s_and_b32 s81, s81, 3\n"
    "s_add_u32 s85, s23, 1\n"
    "s_and_b32 s85, s85, 3\n"
    "s_add_u32 s82, s22, 2\n"
    "s_and_b32 s82, s82, 3\n"
    "s_add_u32 s86, s23, 2\n"
    "s_and_b32 s86, s86, 3\n"
    "s_add_u32 s83, s22, 3\n"
    "s_and_b32 s83, s83, 3\n"
    "s_add_u32 s87, s23, 3\n"
    "s_and_b32 s87, s87, 3\n"
    "s_lshl_b32 s61, s31, 2\n"
    "s_add_u32 s61, s61, s28\n"
    "s_lshl_b32 s61, s61, 3\n"
    "s_add_u32 s61, s61, s30\n"
    "s_lshl_b32 s64, s61, 7\n"
    "s_add_u32 s64, s64, 4096\n"
    "s_lshl_b32 s65, s61, 2\n"
    "s_add_u32 s65, s65, s56\n"
    "s_mul_i32 s65, s65, 18432\n"
    "s_add_u32 s65, s65, 65536\n"
    "s_waitcnt vmcnt(8)\n"
    "s_barrier\n"
    "s_add_u32 m0, s59, 6144\n"
    "s_lshl_b32 s62, s71, 7\n"
    "buffer_load_dwordx4 v216, s[40:43], s62 offen offset:0 lds\n"
    "buffer_load_dwordx4 v217, s[40:43], s62 offen offset:1024 lds\n"
    "buffer_load_dwordx4 v218, s[40:43], s62 offen offset:2048 lds\n"
    "s_add_u32 m0, s59, 9216\n"
    "s_nop 0\n"
    "buffer_load_dwordx4 v219, s[40:43], s62 offen offset:0 lds\n"
    "buffer_load_dwordx4 v220, s[40:43], s62 offen offset:1024 lds\n"
    "buffer_load_dwordx4 v221, s[40:43], s62 offen offset:2048 lds\n"
    "s_add_u32 m0, s58, 49152\n"
    "s_lshl_b32 s62, s71, 19\n"
    "s_add_u32 s62, s62, 262144\n"
    "buffer_load_dwordx4 v212, s[36:39], s62 offen offset:0 lds\n"
    "buffer_load_dwordx4 v213, s[36:39], s62 offen offset:1024 lds\n"
    "buffer_load_dwordx4 v214, s[36:39], s62 offen offset:2048 lds\n"
    "buffer_load_dwordx4 v215, s[36:39], s62 offen offset:3072 lds\n"
    "s_waitcnt vmcnt(14)\n"
    "s_barrier\n"
    "s_add_u32 m0, s59, 0\n"
    "s_lshl_b32 s62, s72, 7\n"
    "buffer_load_dwordx4 v216, s[40:43], s62 offen offset:0 lds\n"
    "buffer_load_dwordx4 v217, s[40:43], s62 offen offset:1024 lds\n"
    "buffer_load_dwordx4 v218, s[40:43], s62 offen offset:2048 lds\n"
    "s_add_u32 m0, s59, 3072\n"
    "s_nop 0\n"
    "buffer_load_dwordx4 v219, s[40:43], s62 offen offset:0 lds\n"
    "buffer_load_dwordx4 v220, s[40:43], s62 offen offset:1024 lds\n"
    "buffer_load_dwordx4 v221, s[40:43], s62 offen offset:2048 lds\n"
    "s_add_u32 m0, s58, 0\n"
    "s_lshl_b32 s62, s72, 19\n"
    "buffer_load_dwordx4 v212, s[36:39], s62 offen offset:0 lds\n"
    "buffer_load_dwordx4 v213, s[36:39], s62 offen offset:1024 lds\n"
    "buffer_load_dwordx4 v214, s[36:39], s62 offen offset:2048 lds\n"
    "buffer_load_dwordx4 v215, s[36:39], s62 offen offset:3072 lds\n"
    "s_waitcnt vmcnt(14)\n"
    "s_barrier\n"
    "s_add_u32 m0, s58, 16384\n"
    "s_lshl_b32 s62, s72, 19\n"
    "s_add_u32 s62, s62, 262144\n"
    "buffer_load_dwordx4 v212, s[36:39], s62 offen offset:0 lds\n"
    "buffer_load_dwordx4 v213, s[36:39], s62 offen offset:1024 lds\n"
    "buffer_load_dwordx4 v214, s[36:39], s62 offen offset:2048 lds\n"
    "buffer_load_dwordx4 v215, s[36:39], s62 offen offset:3072 lds\n"
    "s_waitcnt vmcnt(14)\n"
    "s_barrier\n"
    "s_add_u32 m0, s58, 32768\n"
    "s_lshl_b32 s62, s73, 19\n"
    "buffer_load_dwordx4 v212, s[36:39], s62 offen offset:0 lds\n"
    "buffer_load_dwordx4 v213, s[36:39], s62 offen offset:1024 lds\n"
    "buffer_load_dwordx4 v214, s[36:39], s62 offen offset:2048 lds\n"
    "buffer_load_dwordx4 v215, s[36:39], s62 offen offset:3072 lds\n"
    "s_add_u32 m0, s59, 12288\n"
    "s_add_u32 s62, s35, 0\n"
    "s_and_b32 s62, s62, 3\n"
    "s_lshl_b32 s62, s62, 7\n"
    "buffer_load_dwordx4 v222, s[88:91], s62 offen offset:0 nt lds\n"
    "buffer_load_dwordx4 v223, s[88:91], s62 offen offset:1024 nt lds\n"
    "buffer_load_dwordx4 v224, s[88:91], s62 offen offset:2048 nt lds\n"
    "s_add_u32 m0, s59, 15360\n"
    "s_nop 0\n"
    "buffer_load_dwordx4 v225, s[88:91], s62 offen offset:0 nt lds\n"
    "buffer_load_dwordx4 v226, s[88:91], s62 offen offset:1024 nt lds\n"
    "buffer_load_dwordx4 v227, s[88:91], s62 offen offset:2048 nt lds\n"
    "s_waitcnt vmcnt(14)\n"
    "s_barrier\n"
    "s_add_u32 m0, s59, 6144\n"
    "s_lshl_b32 s62, s73, 7\n"
    "buffer_load_dwordx4 v216, s[40:43], s62 offen offset:0 lds\n"
    "buffer_load_dwordx4 v217, s[40:43], s62 offen offset:1024 lds\n"
    "buffer_load_dwordx4 v218, s[40:43], s62 offen offset:2048 lds\n"
    "s_add_u32 m0, s59, 9216\n"
    "s_nop 0\n"
    "buffer_load_dwordx4 v219, s[40:43], s62 offen offset:0 lds\n"
    "buffer_load_dwordx4 v220, s[40:43], s62 offen offset:1024 lds\n"
    "buffer_load_dwordx4 v221, s[40:43], s62 offen offset:2048 lds\n"
    "s_add_u32 m0, s58, 49152\n"
    "s_lshl_b32 s62, s73, 19\n"
    "s_add_u32 s62, s62, 262144\n"
    "buffer_load_dwordx4 v212, s[36:39], s62 offen offset:0 lds\n"
    "buffer_load_dwordx4 v213, s[36:39], s62 offen offset:1024 lds\n"
    "buffer_load_dwordx4 v214, s[36:39], s62 offen offset:2048 lds\n"
    "buffer_load_dwordx4 v215, s[36:39], s62 offen offset:3072 lds\n"
    "s_waitcnt vmcnt(20)\n"
    "s_barrier\n"
    "s_add_u32 m0, s59, 0\n"
    "s_lshl_b32 s62, s74, 7\n"
    "buffer_load_dwordx4 v216, s[40:43], s62 offen offset:0 lds\n"
    "buffer_load_dwordx4 v217, s[40:43], s62 offen offset:1024 lds\n"
    "buffer_load_dwordx4 v218, s[40:43], s62 offen offset:2048 lds\n"
    "s_add_u32 m0, s59, 3072\n"
    "s_nop 0\n"
    "buffer_load_dwordx4 v219, s[40:43], s62 offen offset:0 lds\n"
    "buffer_load_dwordx4 v220, s[40:43], s62 offen offset:1024 lds\n"
    "buffer_load_dwordx4 v221, s[40:43], s62 offen offset:2048 lds\n"
    "s_add_u32 m0, s58, 0\n"
    "s_lshl_b32 s62, s74, 19\n"
    "buffer_load_dwordx4 v212, s[36:39], s62 offen offset:0 lds\n"
    "buffer_load_dwordx4 v213, s[36:39], s62 offen offset:1024 lds\n"
    "buffer_load_dwordx4 v214, s[36:39], s62 offen offset:2048 lds\n"
    "buffer_load_dwordx4 v215, s[36:39], s62 offen offset:3072 lds\n"
    "s_waitcnt vmcnt(14)\n"
    "s_barrier\n"
    "s_add_u32 m0, s58, 16384\n"
    "s_lshl_b32 s62, s74, 19\n"
    "s_add_u32 s62, s62, 262144\n"
    "buffer_load_dwordx4 v212, s[36:39], s62 offen offset:0 lds\n"
    "buffer_load_dwordx4 v213, s[36:39], s62 offen offset:1024 lds\n"
    "buffer_load_dwordx4 v214, s[36:39], s62 offen offset:2048 lds\n"
    "buffer_load_dwordx4 v215, s[36:39], s62 offen offset:3072 lds\n"
    "s_add_u32 m0, s59, 18432\n"
    "s_add_u32 s62, s35, 1\n"
    "s_and_b32 s62, s62, 3\n"
    "s_lshl_b32 s62, s62, 7\n"
    "buffer_load_dwordx4 v222, s[88:91], s62 offen offset:0 nt lds\n"
    "buffer_load_dwordx4 v223, s[88:91], s62 offen offset:1024 nt lds\n"
    "buffer_load_dwordx4 v224, s[88:91], s62 offen offset:2048 nt lds\n"
    "s_add_u32 m0, s59, 21504\n"
    "s_nop 0\n"
    "buffer_load_dwordx4 v225, s[88:91], s62 offen offset:0 nt lds\n"
    "buffer_load_dwordx4 v226, s[88:91], s62 offen offset:1024 nt lds\n"
    "buffer_load_dwordx4 v227, s[88:91], s62 offen offset:2048 nt lds\n"
    "s_waitcnt vmcnt(20)\n"
    "s_barrier\n"
    "s_add_u32 m0, s59, 6144\n"
    "s_lshl_b32 s62, s75, 7\n"
    "buffer_load_dwordx4 v216, s[40:43], s62 offen offset:0 lds\n"
    "buffer_load_dwordx4 v217, s[40:43], s62 offen offset:1024 lds\n"
    "buffer_load_dwordx4 v218, s[40:43], s62 offen offset:2048 lds\n"
    "s_add_u32 m0, s59, 9216\n"
    "s_nop 0\n"
    "buffer_load_dwordx4 v219, s[40:43], s62 offen offset:0 lds\n"
    "buffer_load_dwordx4 v220, s[40:43], s62 offen offset:1024 lds\n"
    "buffer_load_dwordx4 v221, s[40:43], s62 offen offset:2048 lds\n"
    "s_add_u32 m0, s58, 32768\n"
    "s_lshl_b32 s62, s75, 19\n"
    "buffer_load_dwordx4 v212, s[36:39], s62 offen offset:0 lds\n"
    "buffer_load_dwordx4 v213, s[36:39], s62 offen offset:1024 lds\n"
    "buffer_load_dwordx4 v214, s[36:39], s62 offen offset:2048 lds\n"
    "buffer_load_dwordx4 v215, s[36:39], s62 offen offset:3072 lds\n"
    "s_waitcnt vmcnt(20)\n"
    "s_barrier\n"
    "s_add_u32 m0, s58, 49152\n"
    "s_lshl_b32 s62, s75, 19\n"
    "s_add_u32 s62, s62, 262144\n"
    "buffer_load_dwordx4 v212, s[36:39], s62 offen offset:0 lds\n"
    "buffer_load_dwordx4 v213, s[36:39], s62 offen offset:1024 lds\n"
    "buffer_load_dwordx4 v214, s[36:39], s62 offen offset:2048 lds\n"
    "buffer_load_dwordx4 v215, s[36:39], s62 offen offset:3072 lds\n"
    "s_waitcnt vmcnt(20)\n"
    "s_barrier\n"
    "s_add_u32 m0, s58, 0\n"
    "s_lshl_b32 s62, s76, 19\n"
    "buffer_load_dwordx4 v212, s[36:39], s62 offen offset:0 lds\n"
    "buffer_load_dwordx4 v213, s[36:39], s62 offen offset:1024 lds\n"
    "buffer_load_dwordx4 v214, s[36:39], s62 offen offset:2048 lds\n"
    "buffer_load_dwordx4 v215, s[36:39], s62 offen offset:3072 lds\n"
    "s_mul_i32 s62, s80, 24576\n"
    "s_lshl_b32 s63, s84, 7\n"
    "s_add_u32 s62, s62, s63\n"
    "s_mov_b32 exec_hi, 0xffff\n"
    "buffer_load_dword v241, v233, s[44:47], s62 offen\n"
    "s_mov_b32 exec_hi, -1\n"
    "s_waitcnt vmcnt(9)\n"
    "s_barrier\n"
    "s_add_u32 m0, s59, 0\n"
    "s_lshl_b32 s62, s76, 7\n"
    "buffer_load_dwordx4 v216, s[40:43], s62 offen offset:0 lds\n"
    "buffer_load_dwordx4 v217, s[40:43], s62 offen offset:1024 lds\n"
    "buffer_load_dwordx4 v218, s[40:43], s62 offen offset:2048 lds\n"
    "s_add_u32 m0, s59, 3072\n"
    "s_nop 0\n"
    "buffer_load_dwordx4 v219, s[40:43], s62 offen offset:0 lds\n"
    "buffer_load_dwordx4 v220, s[40:43], s62 offen offset:1024 lds\n"
    "buffer_load_dwordx4 v221, s[40:43], s62 offen offset:2048 lds\n"
    "s_add_u32 m0, s58, 16384\n"
    "s_lshl_b32 s62, s76, 19\n"
    "s_add_u32 s62, s62, 262144\n"
    "buffer_load_dwordx4 v212, s[36:39], s62 offen offset:0 lds\n"
    "buffer_load_dwordx4 v213, s[36:39], s62 offen offset:1024 lds\n"
    "buffer_load_dwordx4 v214, s[36:39], s62 offen offset:2048 lds\n"
    "buffer_load_dwordx4 v215, s[36:39], s62 offen offset:3072 lds\n"
    "s_waitcnt vmcnt(15)\n"
    "s_barrier\n"
    "s_add_u32 m0, s59, 6144\n"
    "s_add_u32 s62, s35, 2\n"
    "s_and_b32 s62, s62, 3\n"
    "s_lshl_b32 s62, s62, 7\n"
    "buffer_load_dwordx4 v222, s[88:91], s62 offen offset:0 nt lds\n"
    "buffer_load_dwordx4 v223, s[88:91], s62 offen offset:1024 nt lds\n"
    "buffer_load_dwordx4 v224, s[88:91], s62 offen offset:2048 nt lds\n"
    "s_add_u32 m0, s59, 9216\n"
    "s_nop 0\n"
    "buffer_load_dwordx4 v225, s[88:91], s62 offen offset:0 nt lds\n"
    "buffer_load_dwordx4 v226, s[88:91], s62 offen offset:1024 nt lds\n"
    "buffer_load_dwordx4 v227, s[88:91], s62 offen offset:2048 nt lds\n"
    "s_add_u32 m0, s60, 32768\n"
    "s_lshl_b32 s62, s80, 2\n"
    "s_add_u32 s62, s62, s84\n"
    "s_mul_i32 s62, s62, 57344\n"
    "s_mov_b32 exec_hi, 0x00ffffff\n"
    "buffer_load_dwordx4 v228, s[48:51], s62 offen offset:0 lds\n"
    "buffer_load_dwordx4 v229, s[48:51], s62 offen offset:896 lds\n"
    "buffer_load_dwordx4 v230, s[48:51], s62 offen offset:1792 lds\n"
    "buffer_load_dwordx4 v231, s[48:51], s62 offen offset:2688 lds\n"
    "s_mov_b32 exec_hi, -1\n"
    "s_waitcnt vmcnt(14)\n"
    "s_barrier\n"
    "s_waitcnt vmcnt(10)\n"
    "s_barrier\n"
    "s_add_u32 m0, s59, 0\n"
    "s_add_u32 s62, s35, 3\n"
    "s_and_b32 s62, s62, 3\n"
    "s_lshl_b32 s62, s62, 7\n"
    "buffer_load_dwordx4 v222, s[88:91], s62 offen offset:0 nt lds\n"
    "buffer_load_dwordx4 v223, s[88:91], s62 offen offset:1024 nt lds\n"
    "buffer_load_dwordx4 v224, s[88:91], s62 offen offset:2048 nt lds\n"
    "s_add_u32 m0, s59, 3072\n"
    "s_nop 0\n"
    "buffer_load_dwordx4 v225, s[88:91], s62 offen offset:0 nt lds\n"
    "buffer_load_dwordx4 v226, s[88:91], s62 offen offset:1024 nt lds\n"
    "buffer_load_dwordx4 v227, s[88:91], s62 offen offset:2048 nt lds\n"
    "s_barrier\n"
    "s_add_u32 m0, s59, 12288\n"
    "s_mul_i32 s62, s80, 24576\n"
    "s_lshl_b32 s63, s84, 7\n"
    "s_add_u32 s62, s62, s63\n"
    "buffer_load_dwordx4 v222, s[44:47], s62 offen offset:0 lds\n"
    "buffer_load_dwordx4 v223, s[44:47], s62 offen offset:1024 lds\n"
    "buffer_load_dwordx4 v224, s[44:47], s62 offen offset:2048 lds\n"
    "s_add_u32 m0, s59, 15360\n"
    "s_nop 0\n"
    "buffer_load_dwordx4 v225, s[44:47], s62 offen offset:0 lds\n"
    "buffer_load_dwordx4 v226, s[44:47], s62 offen offset:1024 lds\n"
    "buffer_load_dwordx4 v227, s[44:47], s62 offen offset:2048 lds\n"
    "s_add_u32 m0, s60, 49152\n"
    "s_lshl_b32 s62, s80, 2\n"
    "s_add_u32 s62, s62, s85\n"
    "s_mul_i32 s62, s62, 57344\n"
    "s_mov_b32 exec_hi, 0x00ffffff\n"
    "buffer_load_dwordx4 v228, s[48:51], s62 offen offset:0 lds\n"
    "buffer_load_dwordx4 v229, s[48:51], s62 offen offset:896 lds\n"
    "buffer_load_dwordx4 v230, s[48:51], s62 offen offset:1792 lds\n"
    "buffer_load_dwordx4 v231, s[48:51], s62 offen offset:2688 lds\n"
    "s_mov_b32 exec_hi, -1\n"
    "s_waitcnt vmcnt(20)\n"
    "s_barrier\n"
    "s_add_u32 m0, s59, 18432\n"
    "s_mul_i32 s62, s80, 24576\n"
    "s_lshl_b32 s63, s85, 7\n"
    "s_add_u32 s62, s62, s63\n"
    "buffer_load_dwordx4 v222, s[44:47], s62 offen offset:0 lds\n"
    "buffer_load_dwordx4 v223, s[44:47], s62 offen offset:1024 lds\n"
    "buffer_load_dwordx4 v224, s[44:47], s62 offen offset:2048 lds\n"
    "s_add_u32 m0, s59, 21504\n"
    "s_nop 0\n"
    "buffer_load_dwordx4 v225, s[44:47], s62 offen offset:0 lds\n"
    "buffer_load_dwordx4 v226, s[44:47], s62 offen offset:1024 lds\n"
    "buffer_load_dwordx4 v227, s[44:47], s62 offen offset:2048 lds\n"
    "s_add_u32 m0, s60, 0\n"
    "s_lshl_b32 s62, s80, 2\n"
    "s_add_u32 s62, s62, s86\n"
    "s_mul_i32 s62, s62, 57344\n"
    "s_mov_b32 exec_hi, 0x00ffffff\n"
    "buffer_load_dwordx4 v228, s[48:51], s62 offen offset:0 lds\n"
    "buffer_load_dwordx4 v229, s[48:51], s62 offen offset:896 lds\n"
    "buffer_load_dwordx4 v230, s[48:51], s62 offen offset:1792 lds\n"
    "buffer_load_dwordx4 v231, s[48:51], s62 offen offset:2688 lds\n"
    "s_mov_b32 exec_hi, -1\n"
    "s_waitcnt vmcnt(20)\n"
    "s_barrier\n"
    "s_add_u32 m0, s59, 6144\n"
    "s_mul_i32 s62, s80, 24576\n"
    "s_lshl_b32 s63, s86, 7\n"
    "s_add_u32 s62, s62, s63\n"
    "buffer_load_dwordx4 v222, s[44:47], s62 offen offset:0 lds\n"
    "buffer_load_dwordx4 v223, s[44:47], s62 offen offset:1024 lds\n"
    "buffer_load_dwordx4 v224, s[44:47], s62 offen offset:2048 lds\n"
    "s_add_u32 m0, s59, 9216\n"
    "s_nop 0\n"
    "buffer_load_dwordx4 v225, s[44:47], s62 offen offset:0 lds\n"
    "buffer_load_dwordx4 v226, s[44:47], s62 offen offset:1024 lds\n"
    "buffer_load_dwordx4 v227, s[44:47], s62 offen offset:2048 lds\n"
    "s_waitcnt vmcnt(20)\n"
    "s_barrier\n"
    "s_add_u32 m0, s59, 0\n"
    "s_mul_i32 s62, s80, 24576\n"
    "s_lshl_b32 s63, s87, 7\n"
    "s_add_u32 s62, s62, s63\n"
    "buffer_load_dwordx4 v222, s[44:47], s62 offen offset:0 lds\n"
    "buffer_load_dwordx4 v223, s[44:47], s62 offen offset:1024 lds\n"
    "buffer_load_dwordx4 v224, s[44:47], s62 offen offset:2048 lds\n"
    "s_add_u32 m0, s59, 3072\n"
    "s_nop 0\n"
    "buffer_load_dwordx4 v225, s[44:47], s62 offen offset:0 lds\n"
    "buffer_load_dwordx4 v226, s[44:47], s62 offen offset:1024 lds\n"
    "buffer_load_dwordx4 v227, s[44:47], s62 offen offset:2048 lds\n"
    "s_add_u32 m0, s60, 16384\n"
    "s_lshl_b32 s62, s80, 2\n"
    "s_add_u32 s62, s62, s87\n"
    "s_mul_i32 s62, s62, 57344\n"
    "s_mov_b32 exec_hi, 0x00ffffff\n"
    "buffer_load_dwordx4 v228, s[48:51], s62 offen offset:0 lds\n"
    "buffer_load_dwordx4 v229, s[48:51], s62 offen offset:896 lds\n"
    "buffer_load_dwordx4 v230, s[48:51], s62 offen offset:1792 lds\n"
    "buffer_load_dwordx4 v231, s[48:51], s62 offen offset:2688 lds\n"
    "s_mov_b32 exec_hi, -1\n"
    "s_waitcnt vmcnt(20)\n"
    "s_barrier\n"
    "s_add_u32 m0, s59, 12288\n"
    "s_mul_i32 s62, s81, 24576\n"
    "s_lshl_b32 s63, s84, 7\n"
    "s_add_u32 s62, s62, s63\n"
    "buffer_load_dwordx4 v222, s[44:47], s62 offen offset:0 lds\n"
    "buffer_load_dwordx4 v223, s[44:47], s62 offen offset:1024 lds\n"
    "buffer_load_dwordx4 v224, s[44:47], s62 offen offset:2048 lds\n"
    "s_add_u32 m0, s59, 15360\n"
    "s_nop 0\n"
    "buffer_load_dwordx4 v225, s[44:47], s62 offen offset:0 lds\n"
    "buffer_load_dwordx4 v226, s[44:47], s62 offen offset:1024 lds\n"
    "buffer_load_dwordx4 v227, s[44:47], s62 offen offset:2048 lds\n"
    "s_add_u32 m0, s60, 32768\n"
    "s_lshl_b32 s62, s81, 2\n"
    "s_add_u32 s62, s62, s84\n"
    "s_mul_i32 s62, s62, 57344\n"
    "s_mov_b32 exec_hi, 0x00ffffff\n"
    "buffer_load_dwordx4 v228, s[48:51], s62 offen offset:0 lds\n"
    "buffer_load_dwordx4 v229, s[48:51], s62 offen offset:896 lds\n"
    "buffer_load_dwordx4 v230, s[48:51], s62 offen offset:1792 lds\n"
    "buffer_load_dwordx4 v231, s[48:51], s62 offen offset:2688 lds\n"
    "s_mov_b32 exec_hi, -1\n"
    "s_waitcnt vmcnt(20)\n"
    "s_barrier\n"
    "s_add_u32 m0, s59, 18432\n"
    "s_mul_i32 s62, s81, 24576\n"
    "s_lshl_b32 s63, s85, 7\n"
    "s_add_u32 s62, s62, s63\n"
    "buffer_load_dwordx4 v222, s[44:47], s62 offen offset:0 lds\n"
    "buffer_load_dwordx4 v223, s[44:47], s62 offen offset:1024 lds\n"
    "buffer_load_dwordx4 v224, s[44:47], s62 offen offset:2048 lds\n"
    "s_add_u32 m0, s59, 21504\n"
    "s_nop 0\n"
    "buffer_load_dwordx4 v225, s[44:47], s62 offen offset:0 lds\n"
    "buffer_load_dwordx4 v226, s[44:47], s62 offen offset:1024 lds\n"
    "buffer_load_dwordx4 v227, s[44:47], s62 offen offset:2048 lds\n"
    "s_add_u32 m0, s60, 49152\n"
    "s_lshl_b32 s62, s81, 2\n"
    "s_add_u32 s62, s62, s85\n"
    "s_mul_i32 s62, s62, 57344\n"
    "s_mov_b32 exec_hi, 0x00ffffff\n"
    "buffer_load_dwordx4 v228, s[48:51], s62 offen offset:0 lds\n"
    "buffer_load_dwordx4 v229, s[48:51], s62 offen offset:896 lds\n"
    "buffer_load_dwordx4 v230, s[48:51], s62 offen offset:1792 lds\n"
    "buffer_load_dwordx4 v231, s[48:51], s62 offen offset:2688 lds\n"
    "s_mov_b32 exec_hi, -1\n"
    "s_waitcnt vmcnt(20)\n"
    "s_barrier\n"
    "s_add_u32 m0, s59, 6144\n"
    "s_mul_i32 s62, s81, 24576\n"
    "s_lshl_b32 s63, s86, 7\n"
    "s_add_u32 s62, s62, s63\n"
    "buffer_load_dwordx4 v222, s[44:47], s62 offen offset:0 lds\n"
    "buffer_load_dwordx4 v223, s[44:47], s62 offen offset:1024 lds\n"
    "buffer_load_dwordx4 v224, s[44:47], s62 offen offset:2048 lds\n"
    "s_add_u32 m0, s59, 9216\n"
    "s_nop 0\n"
    "buffer_load_dwordx4 v225, s[44:47], s62 offen offset:0 lds\n"
    "buffer_load_dwordx4 v226, s[44:47], s62 offen offset:1024 lds\n"
    "buffer_load_dwordx4 v227, s[44:47], s62 offen offset:2048 lds\n"
    "s_add_u32 m0, s60, 0\n"
    "s_lshl_b32 s62, s81, 2\n"
    "s_add_u32 s62, s62, s86\n"
    "s_mul_i32 s62, s62, 57344\n"
    "s_mov_b32 exec_hi, 0x00ffffff\n"
    "buffer_load_dwordx4 v228, s[48:51], s62 offen offset:0 lds\n"
    "buffer_load_dwordx4 v229, s[48:51], s62 offen offset:896 lds\n"
    "buffer_load_dwordx4 v230, s[48:51], s62 offen offset:1792 lds\n"
    "buffer_load_dwordx4 v231, s[48:51], s62 offen offset:2688 lds\n"
    "s_mov_b32 exec_hi, -1\n"
    "s_waitcnt vmcnt(20)\n"
    "s_barrier\n"
    "s_waitcnt lgkmcnt(0)\n"
    "s_add_u32 s52, s52, 0x9E3779B9\n"
    "s_add_u32 s53, s53, 0x85EBCA6B\n"
    "v_mov_b32 v236, s52\n"
    "v_mov_b32 v237, s53\n"
    "s_lshl_b32 s62, s29, 2\n"
    "s_add_u32 s62, s62, s56\n"
    "s_lshl_b32 s62, s62, 3\n"
    "s_add_u32 s62, s62, s64\n"
    "s_mov_b64 exec, 1\n"
    "buffer_store_dwordx2 v[236:237], off, s[92:95], s62 sc1\n"
    "s_mov_b64 exec, -1\n"
    "s_add_u32 m0, s59, 0\n"
    "s_mul_i32 s62, s81, 24576\n"
    "s_lshl_b32 s63, s87, 7\n"
    "s_add_u32 s62, s62, s63\n"
    "buffer_load_dwordx4 v222, s[44:47], s62 offen offset:0 lds\n"
    "buffer_load_dwordx4 v223, s[44:47], s62 offen offset:1024 lds\n"
    "buffer_load_dwordx4 v224, s[44:47], s62 offen offset:2048 lds\n"
    "s_add_u32 m0, s59, 3072\n"
    "s_nop 0\n"
    "buffer_load_dwordx4 v225, s[44:47], s62 offen offset:0 lds\n"
    "buffer_load_dwordx4 v226, s[44:47], s62 offen offset:1024 lds\n"
    "buffer_load_dwordx4 v227, s[44:47], s62 offen offset:2048 lds\n"
    "s_add_u32 m0, s60, 16384\n"
    "s_lshl_b32 s62, s81, 2\n"
    "s_add_u32 s62, s62, s87\n"
    "s_mul_i32 s62, s62, 57344\n"
    "s_mov_b32 exec_hi, 0x00ffffff\n"
    "buffer_load_dwordx4 v228, s[48:51], s62 offen offset:0 lds\n"
    "buffer_load_dwordx4 v229, s[48:51], s62 offen offset:896 lds\n"
    "buffer_load_dwordx4 v230, s[48:51], s62 offen offset:1792 lds\n"
    "buffer_load_dwordx4 v231, s[48:51], s62 offen offset:2688 lds\n"
    "s_mov_b32 exec_hi, -1\n"
    "s_waitcnt vmcnt(21)\n"
    "s_barrier\n"
    "s_add_u32 m0, s59, 12288\n"
    "s_mul_i32 s62, s82, 24576\n"
    "s_lshl_b32 s63, s84, 7\n"
    "s_add_u32 s62, s62, s63\n"
    "buffer_load_dwordx4 v222, s[44:47], s62 offen offset:0 lds\n"
    "buffer_load_dwordx4 v223, s[44:47], s62 offen offset:1024 lds\n"
    "buffer_load_dwordx4 v224, s[44:47], s62 offen offset:2048 lds\n"
    "s_add_u32 m0, s59, 15360\n"
    "s_nop 0\n"
    "buffer_load_dwordx4 v225, s[44:47], s62 offen offset:0 lds\n"
    "buffer_load_dwordx4 v226, s[44:47], s62 offen offset:1024 lds\n"
    "buffer_load_dwordx4 v227, s[44:47], s62 offen offset:2048 lds\n"
    "s_add_u32 m0, s60, 32768\n"
    "s_lshl_b32 s62, s82, 2\n"
    "s_add_u32 s62, s62, s84\n"
    "s_mul_i32 s62, s62, 57344\n"
    "s_mov_b32 exec_hi, 0x00ffffff\n"
    "buffer_load_dwordx4 v228, s[48:51], s62 offen offset:0 lds\n"
    "buffer_load_dwordx4 v229, s[48:51], s62 offen offset:896 lds\n"
    "buffer_load_dwordx4 v230, s[48:51], s62 offen offset:1792 lds\n"
    "buffer_load_dwordx4 v231, s[48:51], s62 offen offset:2688 lds\n"
    "s_mov_b32 exec_hi, -1\n"
    "s_waitcnt vmcnt(21)\n"
    "s_barrier\n"
    "s_add_u32 m0, s59, 18432\n"
    "s_mul_i32 s62, s82, 24576\n"
    "s_lshl_b32 s63, s85, 7\n"
    "s_add_u32 s62, s62, s63\n"
    "buffer_load_dwordx4 v222, s[44:47], s62 offen offset:0 lds\n"
    "buffer_load_dwordx4 v223, s[44:47], s62 offen offset:1024 lds\n"
    "buffer_load_dwordx4 v224, s[44:47], s62 offen offset:2048 lds\n"
    "s_add_u32 m0, s59, 21504\n"
    "s_nop 0\n"
    "buffer_load_dwordx4 v225, s[44:47], s62 offen offset:0 lds\n"
    "buffer_load_dwordx4 v226, s[44:47], s62 offen offset:1024 lds\n"
    "buffer_load_dwordx4 v227, s[44:47], s62 offen offset:2048 lds\n"
    "s_add_u32 m0, s60, 49152\n"
    "s_lshl_b32 s62, s82, 2\n"
    "s_add_u32 s62, s62, s85\n"
    "s_mul_i32 s62, s62, 57344\n"
    "s_mov_b32 exec_hi, 0x00ffffff\n"
    "buffer_load_dwordx4 v228, s[48:51], s62 offen offset:0 lds\n"
    "buffer_load_dwordx4 v229, s[48:51], s62 offen offset:896 lds\n"
    "buffer_load_dwordx4 v230, s[48:51], s62 offen offset:1792 lds\n"
    "buffer_load_dwordx4 v231, s[48:51], s62 offen offset:2688 lds\n"
    "s_mov_b32 exec_hi, -1\n"
    "s_waitcnt vmcnt(20)\n"
    "s_barrier\n"
    "s_add_u32 m0, s59, 6144\n"
    "s_mul_i32 s62, s82, 24576\n"
    "s_lshl_b32 s63, s86, 7\n"
    "s_add_u32 s62, s62, s63\n"
    "buffer_load_dwordx4 v222, s[44:47], s62 offen offset:0 lds\n"
    "buffer_load_dwordx4 v223, s[44:47], s62 offen offset:1024 lds\n"
    "buffer_load_dwordx4 v224, s[44:47], s62 offen offset:2048 lds\n"
    "s_add_u32 m0, s59, 9216\n"
    "s_nop 0\n"
    "buffer_load_dwordx4 v225, s[44:47], s62 offen offset:0 lds\n"
    "buffer_load_dwordx4 v226, s[44:47], s62 offen offset:1024 lds\n"
    "buffer_load_dwordx4 v227, s[44:47], s62 offen offset:2048 lds\n"
    "s_add_u32 m0, s60, 0\n"
    "s_lshl_b32 s62, s82, 2\n"
    "s_add_u32 s62, s62, s86\n"
    "s_mul_i32 s62, s62, 57344\n"
    "s_mov_b32 exec_hi, 0x00ffffff\n"
    "buffer_load_dwordx4 v228, s[48:51], s62 offen offset:0 lds\n"
    "buffer_load_dwordx4 v229, s[48:51], s62 offen offset:896 lds\n"
    "buffer_load_dwordx4 v230, s[48:51], s62 offen offset:1792 lds\n"
    "buffer_load_dwordx4 v231, s[48:51], s62 offen offset:2688 lds\n"
    "s_mov_b32 exec_hi, -1\n"
    "s_waitcnt vmcnt(20)\n"
    "s_barrier\n"
    "s_mov_b64 exec, 0xffff\n"
    "buffer_load_dwordx2 v[238:239], v240, s[92:95], s64 offen sc1\n"
    "s_mov_b64 exec, -1\n"
    "s_add_u32 m0, s59, 0\n"
    "s_mul_i32 s62, s82, 24576\n"
    "s_lshl_b32 s63, s87, 7\n"
    "s_add_u32 s62, s62, s63\n"
    "buffer_load_dwordx4 v222, s[44:47], s62 offen offset:0 lds\n"
    "buffer_load_dwordx4 v223, s[44:47], s62 offen offset:1024 lds\n"
    "buffer_load_dwordx4 v224, s[44:47], s62 offen offset:2048 lds\n"
    "s_add_u32 m0, s59, 3072\n"
    "s_nop 0\n"
    "buffer_load_dwordx4 v225, s[44:47], s62 offen offset:0 lds\n"
    "buffer_load_dwordx4 v226, s[44:47], s62 offen offset:1024 lds\n"
    "buffer_load_dwordx4 v227, s[44:47], s62 offen offset:2048 lds\n"
    "s_add_u32 m0, s60, 16384\n"
    "s_lshl_b32 s62, s82, 2\n"
    "s_add_u32 s62, s62, s87\n"
    "s_mul_i32 s62, s62, 57344\n"
    "s_mov_b32 exec_hi, 0x00ffffff\n"
    "buffer_load_dwordx4 v228, s[48:51], s62 offen offset:0 lds\n"
    "buffer_load_dwordx4 v229, s[48:51], s62 offen offset:896 lds\n"
    "buffer_load_dwordx4 v230, s[48:51], s62 offen offset:1792 lds\n"
    "buffer_load_dwordx4 v231, s[48:51], s62 offen offset:2688 lds\n"
    "s_mov_b32 exec_hi, -1\n"
    "s_waitcnt vmcnt(21)\n"
    "s_barrier\n"
    "s_mov_b64 exec, 0xffff\n"
    "s_mov_b32 s67, 65536\n"
    "s_waitcnt vmcnt(10)\n"
    "v_cmp_ne_u64 vcc, v[236:237], v[238:239]\n"
    "s_nop 0\n"
    "s_cbranch_vccz L0_pok\n"
    "L0_ppoll:\n"
    "buffer_load_dwordx2 v[238:239], v240, s[92:95], s64 offen sc1\n"
    "s_waitcnt vmcnt(0)\n"
    "v_cmp_ne_u64 vcc, v[236:237], v[238:239]\n"
    "s_sub_u32 s67, s67, 1\n"
    "s_cbranch_scc1 L0_pok\n"
    "s_cbranch_vccnz L0_ppoll\n"
    "L0_pok:\n"
    "s_mov_b64 exec, -1\n"
    "s_add_u32 m0, s59, 12288\n"
    "s_add_u32 s62, s65, 0\n"
    "buffer_load_dwordx4 v232, s[92:95], s62 offen offset:0 sc1 lds\n"
    "buffer_load_dwordx4 v232, s[92:95], s62 offen offset:1024 sc1 lds\n"
    "buffer_load_dwordx4 v232, s[92:95], s62 offen offset:2048 sc1 lds\n"
    "s_add_u32 m0, s59, 15360\n"
    "s_add_u32 s62, s65, 3072\n"
    "buffer_load_dwordx4 v232, s[92:95], s62 offen offset:0 sc1 lds\n"
    "buffer_load_dwordx4 v232, s[92:95], s62 offen offset:1024 sc1 lds\n"
    "buffer_load_dwordx4 v232, s[92:95], s62 offen offset:2048 sc1 lds\n"
    "s_add_u32 m0, s60, 32768\n"
    "s_lshl_b32 s62, s83, 2\n"
    "s_add_u32 s62, s62, s84\n"
    "s_mul_i32 s62, s62, 57344\n"
    "s_mov_b32 exec_hi, 0x00ffffff\n"
    "buffer_load_dwordx4 v228, s[48:51], s62 offen offset:0 lds\n"
    "buffer_load_dwordx4 v229, s[48:51], s62 offen offset:896 lds\n"
    "buffer_load_dwordx4 v230, s[48:51], s62 offen offset:1792 lds\n"
    "buffer_load_dwordx4 v231, s[48:51], s62 offen offset:2688 lds\n"
    "s_mov_b32 exec_hi, -1\n"
    "s_barrier\n"
    "s_add_u32 m0, s59, 18432\n"
    "s_add_u32 s62, s65, 6144\n"
    "buffer_load_dwordx4 v232, s[92:95], s62 offen offset:0 sc1 lds\n"
    "buffer_load_dwordx4 v232, s[92:95], s62 offen offset:1024 sc1 lds\n"
    "buffer_load_dwordx4 v232, s[92:95], s62 offen offset:2048 sc1 lds\n"
    "s_add_u32 m0, s59, 21504\n"
    "s_add_u32 s62, s65, 9216\n"
    "buffer_load_dwordx4 v232, s[92:95], s62 offen offset:0 sc1 lds\n"
    "buffer_load_dwordx4 v232, s[92:95], s62 offen offset:1024 sc1 lds\n"
    "buffer_load_dwordx4 v232, s[92:95], s62 offen offset:2048 sc1 lds\n"
    "s_add_u32 m0, s60, 49152\n"
    "s_lshl_b32 s62, s83, 2\n"
    "s_add_u32 s62, s62, s85\n"
    "s_mul_i32 s62, s62, 57344\n"
    "s_mov_b32 exec_hi, 0x00ffffff\n"
    "buffer_load_dwordx4 v228, s[48:51], s62 offen offset:0 lds\n"
    "buffer_load_dwordx4 v229, s[48:51], s62 offen offset:896 lds\n"
    "buffer_load_dwordx4 v230, s[48:51], s62 offen offset:1792 lds\n"
    "buffer_load_dwordx4 v231, s[48:51], s62 offen offset:2688 lds\n"
    "s_mov_b32 exec_hi, -1\n"
    "s_waitcnt vmcnt(20)\n"
    "s_barrier\n"
    "s_add_u32 m0, s59, 6144\n"
    "s_mul_i32 s62, s83, 24576\n"
    "s_lshl_b32 s63, s84, 7\n"
    "s_add_u32 s62, s62, s63\n"
    "buffer_load_dwordx4 v222, s[44:47], s62 offen offset:0 lds\n"
    "buffer_load_dwordx4 v223, s[44:47], s62 offen offset:1024 lds\n"
    "buffer_load_dwordx4 v224, s[44:47], s62 offen offset:2048 lds\n"
    "s_add_u32 m0, s59, 9216\n"
    "s_nop 0\n"
    "buffer_load_dwordx4 v225, s[44:47], s62 offen offset:0 lds\n"
    "buffer_load_dwordx4 v226, s[44:47], s62 offen offset:1024 lds\n"
    "buffer_load_dwordx4 v227, s[44:47], s62 offen offset:2048 lds\n"
    "s_add_u32 m0, s60, 0\n"
    "s_lshl_b32 s62, s83, 2\n"
    "s_add_u32 s62, s62, s86\n"
    "s_mul_i32 s62, s62, 57344\n"
    "s_mov_b32 exec_hi, 0x00ffffff\n"
    "buffer_load_dwordx4 v228, s[48:51], s62 offen offset:0 lds\n"
    "buffer_load_dwordx4 v229, s[48:51], s62 offen offset:896 lds\n"
    "buffer_load_dwordx4 v230, s[48:51], s62 offen offset:1792 lds\n"
    "buffer_load_dwordx4 v231, s[48:51], s62 offen offset:2688 lds\n"
    "s_mov_b32 exec_hi, -1\n"
    "s_waitcnt vmcnt(14)\n"
    "s_barrier\n"
    "s_add_u32 m0, s59, 0\n"
    "s_add_u32 s62, s65, 12288\n"
    "buffer_load_dwordx4 v232, s[92:95], s62 offen offset:0 sc1 lds\n"
    "buffer_load_dwordx4 v232, s[92:95], s62 offen offset:1024 sc1 lds\n"
    "buffer_load_dwordx4 v232, s[92:95], s62 offen offset:2048 sc1 lds\n"
    "s_add_u32 m0, s59, 3072\n"
    "s_add_u32 s62, s65, 15360\n"
    "buffer_load_dwordx4 v232, s[92:95], s62 offen offset:0 sc1 lds\n"
    "buffer_load_dwordx4 v232, s[92:95], s62 offen offset:1024 sc1 lds\n"
    "buffer_load_dwordx4 v232, s[92:95], s62 offen offset:2048 sc1 lds\n"
    "s_barrier\n"
    "s_add_u32 m0, s59, 12288\n"
    "s_mul_i32 s62, s83, 24576\n"
    "s_lshl_b32 s63, s85, 7\n"
    "s_add_u32 s62, s62, s63\n"
    "buffer_load_dwordx4 v222, s[44:47], s62 offen offset:0 lds\n"
    "buffer_load_dwordx4 v223, s[44:47], s62 offen offset:1024 lds\n"
    "buffer_load_dwordx4 v224, s[44:47], s62 offen offset:2048 lds\n"
    "s_add_u32 m0, s59, 15360\n"
    "s_nop 0\n"
    "buffer_load_dwordx4 v225, s[44:47], s62 offen offset:0 lds\n"
    "buffer_load_dwordx4 v226, s[44:47], s62 offen offset:1024 lds\n"
    "buffer_load_dwordx4 v227, s[44:47], s62 offen offset:2048 lds\n"
    "s_add_u32 m0, s59, 18432\n"
    "s_mul_i32 s62, s83, 24576\n"
    "s_lshl_b32 s63, s86, 7\n"
    "s_add_u32 s62, s62, s63\n"
    "buffer_load_dwordx4 v222, s[44:47], s62 offen offset:0 lds\n"
    "buffer_load_dwordx4 v223, s[44:47], s62 offen offset:1024 lds\n"
    "buffer_load_dwordx4 v224, s[44:47], s62 offen offset:2048 lds\n"
    "s_add_u32 m0, s59, 21504\n"
    "s_nop 0\n"
    "buffer_load_dwordx4 v225, s[44:47], s62 offen offset:0 lds\n"
    "buffer_load_dwordx4 v226, s[44:47], s62 offen offset:1024 lds\n"
    "buffer_load_dwordx4 v227, s[44:47], s62 offen offset:2048 lds\n"
    "s_waitcnt vmcnt(12)\n"
    "s_barrier\n"
    "s_add_u32 m0, s60, 16384\n"
    "s_lshl_b32 s62, s83, 2\n"
    "s_add_u32 s62, s62, s87\n"
    "s_mul_i32 s62, s62, 57344\n"
    "s_mov_b32 exec_hi, 0x00ffffff\n"
    "buffer_load_dwordx4 v228, s[48:51], s62 offen offset:0 lds\n"
    "buffer_load_dwordx4 v229, s[48:51], s62 offen offset:896 lds\n"
    "buffer_load_dwordx4 v230, s[48:51], s62 offen offset:1792 lds\n"
    "buffer_load_dwordx4 v231, s[48:51], s62 offen offset:2688 lds\n"
    "s_mov_b32 exec_hi, -1\n"
    "s_waitcnt vmcnt(4)\n"
    "s_barrier\n"
    "s_add_u32 m0, s59, 0\n"
    "s_mul_i32 s62, s83, 24576\n"
    "s_lshl_b32 s63, s87, 7\n"
    "s_add_u32 s62, s62, s63\n"
    "buffer_load_dwordx4 v222, s[44:47], s62 offen offset:0 lds\n"
    "buffer_load_dwordx4 v223, s[44:47], s62 offen offset:1024 lds\n"
    "buffer_load_dwordx4 v224, s[44:47], s62 offen offset:2048 lds\n"
    "s_add_u32 m0, s59, 3072\n"
    "s_nop 0\n"
    "buffer_load_dwordx4 v225, s[44:47], s62 offen offset:0 lds\n"
    "buffer_load_dwordx4 v226, s[44:47], s62 offen offset:1024 lds\n"
    "buffer_load_dwordx4 v227, s[44:47], s62 offen offset:2048 lds\n"
    "s_waitcnt vmcnt(0)\n"
    "s_barrier\n"
    "s_branch L_end\n"
    "L_cons:\n"
    "s_and_b32 s22, s30, 1\n"
    "s_lshr_b32 s23, s30, 1\n"
    "s_add_u32 s23, s23, s29\n"
    "s_and_b32 s23, s23, 3\n"
    "s_mov_b32 s34, 0x1065432\n"
    "s_and_b32 s57, s29, 1\n"
    "s_lshl_b32 s57, s57, 2\n"
    "s_add_u32 s57, s57, s28\n"
    "s_cmp_eq_u32 s57, 1\n"
    "s_cselect_b32 s34, 0x3210654, s34\n"
    "s_cmp_eq_u32 s57, 2\n"
    "s_cselect_b32 s34, 0x5432106, s34\n"
    "s_cmp_eq_u32 s57, 3\n"
    "s_cselect_b32 s34, 0x6543210, s34\n"
    "s_cmp_eq_u32 s57, 4\n"
    "s_cselect_b32 s34, 0x2106543, s34\n"
    "s_cmp_eq_u32 s57, 5\n"
    "s_cselect_b32 s34, 0x4321065, s34\n"
    "s_cmp_eq_u32 s57, 6\n"
    "s_cselect_b32 s34, 0x6543210, s34\n"
    "s_cmp_eq_u32 s57, 7\n"
    "s_cselect_b32 s34, 0x0654321, s34\n"
    "s_waitcnt lgkmcnt(0)\n"
    "s_lshl_b32 s54, s31, 3\n"
    "s_add_u32 s54, s54, s30\n"
    "s_lshl_b32 s55, s54, 7\n"
    "s_load_dwordx2 s[52:53], s[26:27], s55\n"
    "s_mul_i32 s60, s30, 344064\n"
    "s_mul_i32 s61, s31, 896\n"
    "s_add_u32 s60, s60, s61\n"
    "s_mul_i32 s61, s29, 224\n"
    "s_add_u32 s60, s60, s61\n"
    "s_add_u32 s24, s24, s60\n"
    "s_addc_u32 s25, s25, 0\n"
    "v_and_b32 v1, 15, v255\n"
    "v_lshrrev_b32 v2, 4, v255\n"
    "v_lshrrev_b32 v3, 2, v255\n"
    "v_and_b32 v4, 3, v255\n"
    "v_and_b32 v5, 7, v3\n"
    "v_lshlrev_b32 v5, 5, v5\n"
    "v_lshl_add_u32 v5, v3, 9, v5\n"
    "v_lshl_add_u32 v244, v4, 3, v5\n"
    "v_xor_b32 v245, 32, v244\n"
    "v_xor_b32 v246, 64, v244\n"
    "v_xor_b32 v247, 96, v244\n"
    "v_xor_b32 v248, 128, v244\n"
    "v_xor_b32 v249, 160, v244\n"
    "v_xor_b32 v250, 192, v244\n"
    "v_xor_b32 v251, 224, v244\n"
    "s_mul_i32 s56, %0, 24576\n"
    "s_add_u32 s56, s56, 65536\n"
    "v_and_b32 v5, 6, v1\n"
    "v_xor_b32 v5, v5, v2\n"
    "v_lshlrev_b32 v5, 4, v5\n"
    "v_lshl_add_u32 v5, v1, 7, v5\n"
    "v_add_u32 v252, s56, v5\n"
    "v_and_b32 v5, 6, v1\n"
    "v_lshrrev_b32 v6, 1, v2\n"
    "v_xor_b32 v5, v5, v6\n"
    "v_lshlrev_b32 v5, 4, v5\n"
    "v_lshl_add_u32 v5, v1, 7, v5\n"
    "v_and_b32 v6, 1, v2\n"
    "v_lshl_add_u32 v5, v6, 3, v5\n"
    "v_add_u32 v254, s56, v5\n"
    "s_mov_b64 s[36:37], s[26:27]\n"
    "s_mov_b32 s38, 20512768\n"
    "s_mov_b32 s39, 0x00020000\n"
    "s_lshl_b32 s57, s31, 2\n"
    "s_add_u32 s57, s57, s28\n"
    "s_lshl_b32 s57, s57, 3\n"
    "s_add_u32 s57, s57, s30\n"
    "s_lshl_b32 s58, s57, 2\n"
    "s_add_u32 s58, s58, %0\n"
    "s_mul_i32 s58, s58, 18432\n"
    "s_add_u32 s58, s58, 65536\n"
    "s_sub_u32 s60, s29, s22\n"
    "s_and_b32 s60, s60, 3\n"
    "s_mul_i32 s77, s60, 1536\n"
    "s_cmp_ge_u32 s60, 2\n"
    "s_cselect_b32 s61, 3072, 0\n"
    "s_add_u32 s77, s77, s61\n"
    "s_add_u32 s77, s77, 9216\n"
    "s_add_u32 s77, s77, s58\n"
    "s_cmp_eq_u32 s60, 3\n"
    "s_cselect_b32 s60, 4, s60\n"
    "s_mul_i32 s60, s60, 3072\n"
    "s_add_u32 s58, s58, s60\n"
    "s_lshl_b32 s61, s54, 2\n"
    "s_add_u32 s61, s61, s29\n"
    "s_lshl_b32 s61, s61, 2\n"
    "s_add_u32 s61, s61, %0\n"
    "s_mul_i32 s62, s61, 57344\n"
    "s_add_u32 s62, s62, 4784128\n"
    "s_bfe_u32 s70, s34, 262144\n"
    "s_bfe_u32 s71, s34, 262148\n"
    "s_bfe_u32 s72, s34, 262152\n"
    "s_bfe_u32 s73, s34, 262156\n"
    "s_bfe_u32 s74, s34, 262160\n"
    "s_bfe_u32 s75, s34, 262164\n"
    "s_bfe_u32 s76, s34, 262168\n"
    "s_mov_b64 s[40:41], s[24:25]\n"
    "s_mul_i32 s60, %0, 86016\n"
    "s_add_u32 s40, s40, s60\n"
    "s_addc_u32 s41, s41, 0\n"
    "s_mov_b32 s42, 84448\n"
    "s_mov_b32 s43, 0x00020000\n"
    "s_mov_b32 s65, 0x3db8aa3b\n"
    "s_mov_b32 s67, 65536\n"
    "s_waitcnt vmcnt(0)\n"
    "v_xor_b32 v253, 64, v252\n"
    "s_waitcnt lgkmcnt(0)\n"
    "s_add_u32 s52, s52, 0x9E3779B9\n"
    "s_add_u32 s53, s53, 0x85EBCA6B\n"
    "s_lshl_b32 s78, s52, 16\n"
    "s_and_b32 s78, s78, 0x9fff0000\n"
    "s_or_b32 s78, s78, 0x40000000\n"
    "s_lshl_b32 s79, s53, 16\n"
    "s_and_b32 s79, s79, 0x9fff0000\n"
    "s_or_b32 s79, s79, 0x40000000\n"
    "s_barrier\n"
    "ds_read_b128 v[192:195], v252 offset:0\n"
    "ds_read_b128 v[196:199], v252 offset:2048\n"
    "ds_read_b128 v[200:203], v252 offset:4096\n"
    "ds_read_b64_tr_b16 v[204:205], v244 offset:0\n"
    "ds_read_b64_tr_b16 v[206:207], v244 offset:8192\n"
    "ds_read_b64_tr_b16 v[208:209], v245 offset:0\n"
    "ds_read_b64_tr_b16 v[210:211], v245 offset:8192\n"
    "ds_read_b64_tr_b16 v[212:213], v246 offset:0\n"
    "ds_read_b64_tr_b16 v[214:215], v246 offset:8192\n"
    "ds_read_b64_tr_b16 v[216:217], v247 offset:0\n"
    "ds_read_b64_tr_b16 v[218:219], v247 offset:8192\n"
    "ds_read_b64_tr_b16 v[220:221], v248 offset:0\n"
    "s_waitcnt lgkmcnt(7)\n"
    "v_mfma_f32_16x16x32_bf16 v[0:3], v[204:207], v[192:195], 0\n"
    "ds_read_b64_tr_b16 v[222:223], v248 offset:8192\n"
    "v_mfma_f32_16x16x32_bf16 v[4:7], v[204:207], v[196:199], 0\n"
    "v_mfma_f32_16x16x32_bf16 v[8:11], v[204:207], v[200:203], 0\n"
    "s_nop 9\n"
    "s_waitcnt lgkmcnt(6)\n"
    "v_mfma_f32_16x16x32_bf16 v[20:23], v[208:211], v[200:203], 0\n"
    "ds_read_b64_tr_b16 v[224:225], v249 offset:0\n"
    "ds_read_b64_tr_b16 v[226:227], v249 offset:8192\n"
    "v_mfma_f32_16x16x32_bf16 v[16:19], v[208:211], v[196:199], 0\n"
    "v_mfma_f32_16x16x32_bf16 v[12:15], v[208:211], v[192:195], 0\n"
    "s_nop 9\n"
    "s_waitcnt lgkmcnt(6)\n"
    "v_mfma_f32_16x16x32_bf16 v[24:27], v[212:215], v[192:195], 0\n"
    "ds_read_b64_tr_b16 v[228:229], v250 offset:0\n"
    "ds_read_b64_tr_b16 v[230:231], v250 offset:8192\n"
    "v_mfma_f32_16x16x32_bf16 v[28:31], v[212:215], v[196:199], 0\n"
    "ds_read_b128 v[232:235], v253 offset:0\n"
    "v_mfma_f32_16x16x32_bf16 v[32:35], v[212:215], v[200:203], 0\n"
    "s_nop 9\n"
    "s_waitcnt lgkmcnt(7)\n"
    "v_mfma_f32_16x16x32_bf16 v[44:47], v[216:219], v[200:203], 0\n"
    "ds_read_b64_tr_b16 v[236:237], v251 offset:0\n"
    "ds_read_b64_tr_b16 v[238:239], v251 offset:8192\n"
    "v_mfma_f32_16x16x32_bf16 v[40:43], v[216:219], v[196:199], 0\n"
    "ds_read_b128 v[240:243], v253 offset:2048\n"
    "v_mfma_f32_16x16x32_bf16 v[36:39], v[216:219], v[192:195], 0\n"
    "s_nop 9\n"
    "s_waitcnt lgkmcnt(8)\n"
    "v_mfma_f32_16x16x32_bf16 v[48:51], v[220:223], v[192:195], 0\n"
    "ds_read_b64_tr_b16 v[204:205], v244 offset:256\n"
    "ds_read_b64_tr_b16 v[206:207], v244 offset:8448\n"
    "v_mfma_f32_16x16x32_bf16 v[52:55], v[220:223], v[196:199], 0\n"
    "ds_read_b128 v[208:211], v253 offset:4096\n"
    "v_mfma_f32_16x16x32_bf16 v[56:59], v[220:223], v[200:203], 0\n"
    "s_nop 9\n"
    "s_waitcnt lgkmcnt(9)\n"
    "v_mfma_f32_16x16x32_bf16 v[68:71], v[224:227], v[200:203], 0\n"
    "ds_read_b64_tr_b16 v[212:213], v245 offset:256\n"
    "ds_read_b64_tr_b16 v[214:215], v245 offset:8448\n"
    "v_mfma_f32_16x16x32_bf16 v[64:67], v[224:227], v[196:199], 0\n"
    "v_mfma_f32_16x16x32_bf16 v[60:63], v[224:227], v[192:195], 0\n"
    "s_nop 9\n"
    "s_waitcnt lgkmcnt(9)\n"
    "v_mfma_f32_16x16x32_bf16 v[72:75], v[228:231], v[192:195], 0\n"
    "ds_read_b64_tr_b16 v[216:217], v246 offset:256\n"
    "ds_read_b64_tr_b16 v[218:219], v246 offset:8448\n"
    "v_mfma_f32_16x16x32_bf16 v[76:79], v[228:231], v[196:199], 0\n"
    "v_mfma_f32_16x16x32_bf16 v[80:83], v[228:231], v[200:203], 0\n"
    "s_nop 9\n"
    "s_waitcnt lgkmcnt(8)\n"
    "v_mfma_f32_16x16x32_bf16 v[92:95], v[236:239], v[200:203], 0\n"
    "ds_read_b64_tr_b16 v[220:221], v247 offset:256\n"
    "ds_read_b64_tr_b16 v[222:223], v247 offset:8448\n"
    "v_mfma_f32_16x16x32_bf16 v[88:91], v[236:239], v[196:199], 0\n"
    "v_mfma_f32_16x16x32_bf16 v[84:87], v[236:239], v[192:195], 0\n"
    "s_nop 9\n"
    "s_waitcnt lgkmcnt(7)\n"
    "v_mfma_f32_16x16x32_bf16 v[96:99], v[204:207], v[192:195], 0\n"
    "ds_read_b64_tr_b16 v[224:225], v248 offset:256\n"
    "ds_read_b64_tr_b16 v[226:227], v248 offset:8448\n"
    "v_mfma_f32_16x16x32_bf16 v[100:103], v[204:207], v[196:199], 0\n"
    "v_mfma_f32_16x16x32_bf16 v[104:107], v[204:207], v[200:203], 0\n"
    "s_nop 9\n"
    "s_waitcnt lgkmcnt(6)\n"
    "v_mfma_f32_16x16x32_bf16 v[116:119], v[212:215], v[200:203], 0\n"
    "ds_read_b64_tr_b16 v[228:229], v249 offset:256\n"
    "ds_read_b64_tr_b16 v[230:231], v249 offset:8448\n"
    "v_mfma_f32_16x16x32_bf16 v[112:115], v[212:215], v[196:199], 0\n"
    "v_mfma_f32_16x16x32_bf16 v[108:111], v[212:215], v[192:195], 0\n"
    "s_nop 9\n"
    "s_waitcnt lgkmcnt(6)\n"
    "v_mfma_f32_16x16x32_bf16 v[120:123], v[216:219], v[192:195], 0\n"
    "ds_read_b64_tr_b16 v[236:237], v250 offset:256\n"
    "ds_read_b64_tr_b16 v[238:239], v250 offset:8448\n"
    "v_mfma_f32_16x16x32_bf16 v[124:127], v[216:219], v[196:199], 0\n"
    "v_mfma_f32_16x16x32_bf16 v[128:131], v[216:219], v[200:203], 0\n"
    "s_nop 9\n"
    "s_waitcnt lgkmcnt(6)\n"
    "v_mfma_f32_16x16x32_bf16 v[140:143], v[220:223], v[200:203], 0\n"
    "ds_read_b64_tr_b16 v[204:205], v251 offset:256\n"
    "ds_read_b64_tr_b16 v[206:207], v251 offset:8448\n"
    "v_mfma_f32_16x16x32_bf16 v[136:139], v[220:223], v[196:199], 0\n"
    "v_mfma_f32_16x16x32_bf16 v[132:135], v[220:223], v[192:195], 0\n"
    "s_nop 9\n"
    "s_waitcnt lgkmcnt(6)\n"
    "v_mfma_f32_16x16x32_bf16 v[144:147], v[224:227], v[192:195], 0\n"
    "v_mfma_f32_16x16x32_bf16 v[148:151], v[224:227], v[196:199], 0\n"
    "v_mfma_f32_16x16x32_bf16 v[152:155], v[224:227], v[200:203], 0\n"
    "s_nop 9\n"
    "s_waitcnt lgkmcnt(0)\n"
    "s_barrier\n"
    "v_mfma_f32_16x16x32_bf16 v[164:167], v[228:231], v[200:203], 0\n"
    "ds_read_b64_tr_b16 v[212:213], v244 offset:16384\n"
    "ds_read_b64_tr_b16 v[214:215], v244 offset:24576\n"
    "v_mfma_f32_16x16x32_bf16 v[160:163], v[228:231], v[196:199], 0\n"
    "ds_read_b64_tr_b16 v[216:217], v245 offset:16384\n"
    "ds_read_b64_tr_b16 v[218:219], v245 offset:24576\n"
    "v_mfma_f32_16x16x32_bf16 v[156:159], v[228:231], v[192:195], 0\n"
    "s_nop 9\n"
    "v_mfma_f32_16x16x32_bf16 v[168:171], v[236:239], v[192:195], 0\n"
    "ds_read_b64_tr_b16 v[220:221], v246 offset:16384\n"
    "ds_read_b64_tr_b16 v[222:223], v246 offset:24576\n"
    "v_mfma_f32_16x16x32_bf16 v[172:175], v[236:239], v[196:199], 0\n"
    "v_mfma_f32_16x16x32_bf16 v[176:179], v[236:239], v[200:203], 0\n"
    "s_nop 9\n"
    "v_mfma_f32_16x16x32_bf16 v[188:191], v[204:207], v[200:203], 0\n"
    "ds_read_b64_tr_b16 v[224:225], v247 offset:16384\n"
    "ds_read_b64_tr_b16 v[226:227], v247 offset:24576\n"
    "v_mfma_f32_16x16x32_bf16 v[184:187], v[204:207], v[196:199], 0\n"
    "v_mfma_f32_16x16x32_bf16 v[180:183], v[204:207], v[192:195], 0\n"
    "s_nop 9\n"
    "s_waitcnt lgkmcnt(6)\n"
    "v_mfma_f32_16x16x32_bf16 v[0:3], v[212:215], v[232:235], v[0:3]\n"
    "ds_read_b64_tr_b16 v[228:229], v248 offset:16384\n"
    "ds_read_b64_tr_b16 v[230:231], v248 offset:24576\n"
    "v_mfma_f32_16x16x32_bf16 v[4:7], v[212:215], v[240:243], v[4:7]\n"
    "v_mfma_f32_16x16x32_bf16 v[8:11], v[212:215], v[208:211], v[8:11]\n"
    "s_nop 7\n"
    "s_waitcnt lgkmcnt(6)\n"
    "v_mfma_f32_16x16x32_bf16 v[20:23], v[216:219], v[208:211], v[20:23]\n"
    "ds_read_b64_tr_b16 v[236:237], v249 offset:16384\n"
    "ds_read_b64_tr_b16 v[238:239], v249 offset:24576\n"
    "v_mfma_f32_16x16x32_bf16 v[16:19], v[216:219], v[240:243], v[16:19]\n"
    "v_mfma_f32_16x16x32_bf16 v[12:15], v[216:219], v[232:235], v[12:15]\n"
    "s_nop 7\n"
    "s_waitcnt lgkmcnt(6)\n"
    "v_mfma_f32_16x16x32_bf16 v[24:27], v[220:223], v[232:235], v[24:27]\n"
    "ds_read_b64_tr_b16 v[204:205], v250 offset:16384\n"
    "ds_read_b64_tr_b16 v[206:207], v250 offset:24576\n"
    "v_mfma_f32_16x16x32_bf16 v[28:31], v[220:223], v[240:243], v[28:31]\n"
    "v_mfma_f32_16x16x32_bf16 v[32:35], v[220:223], v[208:211], v[32:35]\n"
    "s_nop 7\n"
    "s_waitcnt lgkmcnt(6)\n"
    "v_mfma_f32_16x16x32_bf16 v[44:47], v[224:227], v[208:211], v[44:47]\n"
    "ds_read_b64_tr_b16 v[192:193], v251 offset:16384\n"
    "ds_read_b64_tr_b16 v[194:195], v251 offset:24576\n"
    "v_mfma_f32_16x16x32_bf16 v[40:43], v[224:227], v[240:243], v[40:43]\n"
    "v_mfma_f32_16x16x32_bf16 v[36:39], v[224:227], v[232:235], v[36:39]\n"
    "s_nop 7\n"
    "s_waitcnt lgkmcnt(6)\n"
    "v_mfma_f32_16x16x32_bf16 v[48:51], v[228:231], v[232:235], v[48:51]\n"
    "ds_read_b64_tr_b16 v[196:197], v244 offset:16640\n"
    "ds_read_b64_tr_b16 v[198:199], v244 offset:24832\n"
    "v_mfma_f32_16x16x32_bf16 v[52:55], v[228:231], v[240:243], v[52:55]\n"
    "v_mfma_f32_16x16x32_bf16 v[56:59], v[228:231], v[208:211], v[56:59]\n"
    "s_nop 7\n"
    "s_waitcnt lgkmcnt(6)\n"
    "v_mfma_f32_16x16x32_bf16 v[68:71], v[236:239], v[208:211], v[68:71]\n"
    "ds_read_b64_tr_b16 v[200:201], v245 offset:16640\n"
    "ds_read_b64_tr_b16 v[202:203], v245 offset:24832\n"
    "v_mfma_f32_16x16x32_bf16 v[64:67], v[236:239], v[240:243], v[64:67]\n"
    "v_mfma_f32_16x16x32_bf16 v[60:63], v[236:239], v[232:235], v[60:63]\n"
    "s_nop 7\n"
    "s_waitcnt lgkmcnt(6)\n"
    "v_mfma_f32_16x16x32_bf16 v[72:75], v[204:207], v[232:235], v[72:75]\n"
    "ds_read_b64_tr_b16 v[212:213], v246 offset:16640\n"
    "ds_read_b64_tr_b16 v[214:215], v246 offset:24832\n"
    "v_mfma_f32_16x16x32_bf16 v[76:79], v[204:207], v[240:243], v[76:79]\n"
    "v_mfma_f32_16x16x32_bf16 v[80:83], v[204:207], v[208:211], v[80:83]\n"
    "s_nop 7\n"
    "s_waitcnt lgkmcnt(6)\n"
    "v_mfma_f32_16x16x32_bf16 v[92:95], v[192:195], v[208:211], v[92:95]\n"
    "ds_read_b64_tr_b16 v[216:217], v247 offset:16640\n"
    "ds_read_b64_tr_b16 v[218:219], v247 offset:24832\n"
    "v_mfma_f32_16x16x32_bf16 v[88:91], v[192:195], v[240:243], v[88:91]\n"
    "v_mfma_f32_16x16x32_bf16 v[84:87], v[192:195], v[232:235], v[84:87]\n"
    "s_nop 7\n"
    "s_waitcnt lgkmcnt(6)\n"
    "v_mfma_f32_16x16x32_bf16 v[96:99], v[196:199], v[232:235], v[96:99]\n"
    "ds_read_b64_tr_b16 v[220:221], v248 offset:16640\n"
    "ds_read_b64_tr_b16 v[222:223], v248 offset:24832\n"
    "v_mfma_f32_16x16x32_bf16 v[100:103], v[196:199], v[240:243], v[100:103]\n"
    "v_mfma_f32_16x16x32_bf16 v[104:107], v[196:199], v[208:211], v[104:107]\n"
    "s_nop 7\n"
    "s_waitcnt lgkmcnt(6)\n"
    "v_mfma_f32_16x16x32_bf16 v[116:119], v[200:203], v[208:211], v[116:119]\n"
    "ds_read_b64_tr_b16 v[224:225], v249 offset:16640\n"
    "ds_read_b64_tr_b16 v[226:227], v249 offset:24832\n"
    "v_mfma_f32_16x16x32_bf16 v[112:115], v[200:203], v[240:243], v[112:115]\n"
    "v_mfma_f32_16x16x32_bf16 v[108:111], v[200:203], v[232:235], v[108:111]\n"
    "s_nop 7\n"
    "s_waitcnt lgkmcnt(6)\n"
    "v_mfma_f32_16x16x32_bf16 v[120:123], v[212:215], v[232:235], v[120:123]\n"
    "ds_read_b64_tr_b16 v[228:229], v250 offset:16640\n"
    "ds_read_b64_tr_b16 v[230:231], v250 offset:24832\n"
    "v_mfma_f32_16x16x32_bf16 v[124:127], v[212:215], v[240:243], v[124:127]\n"
    "v_mfma_f32_16x16x32_bf16 v[128:131], v[212:215], v[208:211], v[128:131]\n"
    "s_nop 7\n"
    "s_waitcnt lgkmcnt(6)\n"
    "v_mfma_f32_16x16x32_bf16 v[140:143], v[216:219], v[208:211], v[140:143]\n"
    "ds_read_b64_tr_b16 v[236:237], v251 offset:16640\n"
    "ds_read_b64_tr_b16 v[238:239], v251 offset:24832\n"
    "v_mfma_f32_16x16x32_bf16 v[136:139], v[216:219], v[240:243], v[136:139]\n"
    "v_mfma_f32_16x16x32_bf16 v[132:135], v[216:219], v[232:235], v[132:135]\n"
    "s_nop 7\n"
    "s_waitcnt lgkmcnt(6)\n"
    "v_mfma_f32_16x16x32_bf16 v[144:147], v[220:223], v[232:235], v[144:147]\n"
    "v_mfma_f32_16x16x32_bf16 v[148:151], v[220:223], v[240:243], v[148:151]\n"
    "v_mfma_f32_16x16x32_bf16 v[152:155], v[220:223], v[208:211], v[152:155]\n"
    "s_nop 7\n"
    "s_waitcnt lgkmcnt(0)\n"
    "s_barrier\n"
    "v_mfma_f32_16x16x32_bf16 v[164:167], v[224:227], v[208:211], v[164:167]\n"
    "ds_read_b64_tr_b16 v[204:205], v244 offset:32768\n"
    "ds_read_b64_tr_b16 v[206:207], v244 offset:40960\n"
    "v_mfma_f32_16x16x32_bf16 v[160:163], v[224:227], v[240:243], v[160:163]\n"
    "ds_read_b128 v[192:195], v252 offset:6144\n"
    "ds_read_b128 v[196:199], v252 offset:8192\n"
    "v_mfma_f32_16x16x32_bf16 v[156:159], v[224:227], v[232:235], v[156:159]\n"
    "ds_read_b128 v[200:203], v252 offset:10240\n"
    "ds_read_b64_tr_b16 v[212:213], v245 offset:32768\n"
    "s_nop 7\n"
    "v_mfma_f32_16x16x32_bf16 v[168:171], v[228:231], v[232:235], v[168:171]\n"
    "ds_read_b64_tr_b16 v[214:215], v245 offset:40960\n"
    "ds_read_b64_tr_b16 v[216:217], v246 offset:32768\n"
    "v_mfma_f32_16x16x32_bf16 v[172:175], v[228:231], v[240:243], v[172:175]\n"
    "ds_read_b64_tr_b16 v[218:219], v246 offset:40960\n"
    "v_mfma_f32_16x16x32_bf16 v[176:179], v[228:231], v[208:211], v[176:179]\n"
    "s_nop 7\n"
    "v_mfma_f32_16x16x32_bf16 v[188:191], v[236:239], v[208:211], v[188:191]\n"
    "ds_read_b64_tr_b16 v[220:221], v247 offset:32768\n"
    "ds_read_b64_tr_b16 v[222:223], v247 offset:40960\n"
    "v_mfma_f32_16x16x32_bf16 v[184:187], v[236:239], v[240:243], v[184:187]\n"
    "v_mfma_f32_16x16x32_bf16 v[180:183], v[236:239], v[232:235], v[180:183]\n"
    "s_nop 7\n"
    "s_waitcnt lgkmcnt(8)\n"
    "v_mfma_f32_16x16x32_bf16 v[0:3], v[204:207], v[192:195], v[0:3]\n"
    "ds_read_b64_tr_b16 v[224:225], v248 offset:32768\n"
    "ds_read_b64_tr_b16 v[226:227], v248 offset:40960\n"
    "s_waitcnt lgkmcnt(9)\n"
    "v_mfma_f32_16x16x32_bf16 v[4:7], v[204:207], v[196:199], v[4:7]\n"
    "s_waitcnt lgkmcnt(8)\n"
    "v_mfma_f32_16x16x32_bf16 v[8:11], v[204:207], v[200:203], v[8:11]\n"
    "s_nop 5\n"
    "s_waitcnt lgkmcnt(6)\n"
    "v_mfma_f32_16x16x32_bf16 v[20:23], v[212:215], v[200:203], v[20:23]\n"
    "ds_read_b64_tr_b16 v[228:229], v249 offset:32768\n"
    "ds_read_b64_tr_b16 v[230:231], v249 offset:40960\n"
    "v_mfma_f32_16x16x32_bf16 v[16:19], v[212:215], v[196:199], v[16:19]\n"
    "v_mfma_f32_16x16x32_bf16 v[12:15], v[212:215], v[192:195], v[12:15]\n"
    "s_nop 5\n"
    "s_waitcnt lgkmcnt(6)\n"
    "v_mfma_f32_16x16x32_bf16 v[24:27], v[216:219], v[192:195], v[24:27]\n"
    "ds_read_b64_tr_b16 v[236:237], v250 offset:32768\n"
    "ds_read_b64_tr_b16 v[238:239], v250 offset:40960\n"
    "v_mfma_f32_16x16x32_bf16 v[28:31], v[216:219], v[196:199], v[28:31]\n"
    "ds_read_b128 v[232:235], v253 offset:6144\n"
    "v_mfma_f32_16x16x32_bf16 v[32:35], v[216:219], v[200:203], v[32:35]\n"
    "s_nop 5\n"
    "s_waitcnt lgkmcnt(7)\n"
    "v_mfma_f32_16x16x32_bf16 v[44:47], v[220:223], v[200:203], v[44:47]\n"
    "ds_read_b64_tr_b16 v[240:241], v251 offset:32768\n"
    "ds_read_b64_tr_b16 v[242:243], v251 offset:40960\n"
    "v_mfma_f32_16x16x32_bf16 v[40:43], v[220:223], v[196:199], v[40:43]\n"
    "ds_read_b128 v[208:211], v253 offset:8192\n"
    "v_mfma_f32_16x16x32_bf16 v[36:39], v[220:223], v[192:195], v[36:39]\n"
    "s_nop 5\n"
    "s_waitcnt lgkmcnt(8)\n"
    "v_mfma_f32_16x16x32_bf16 v[48:51], v[224:227], v[192:195], v[48:51]\n"
    "ds_read_b64_tr_b16 v[204:205], v244 offset:33024\n"
    "ds_read_b64_tr_b16 v[206:207], v244 offset:41216\n"
    "v_mfma_f32_16x16x32_bf16 v[52:55], v[224:227], v[196:199], v[52:55]\n"
    "ds_read_b128 v[212:215], v253 offset:10240\n"
    "v_mfma_f32_16x16x32_bf16 v[56:59], v[224:227], v[200:203], v[56:59]\n"
    "s_nop 5\n"
    "s_waitcnt lgkmcnt(9)\n"
    "v_mfma_f32_16x16x32_bf16 v[68:71], v[228:231], v[200:203], v[68:71]\n"
    "ds_read_b64_tr_b16 v[216:217], v245 offset:33024\n"
    "ds_read_b64_tr_b16 v[218:219], v245 offset:41216\n"
    "v_mfma_f32_16x16x32_bf16 v[64:67], v[228:231], v[196:199], v[64:67]\n"
    "v_mfma_f32_16x16x32_bf16 v[60:63], v[228:231], v[192:195], v[60:63]\n"
    "s_nop 5\n"
    "s_waitcnt lgkmcnt(9)\n"
    "v_mfma_f32_16x16x32_bf16 v[72:75], v[236:239], v[192:195], v[72:75]\n"
    "ds_read_b64_tr_b16 v[220:221], v246 offset:33024\n"
    "ds_read_b64_tr_b16 v[222:223], v246 offset:41216\n"
    "v_mfma_f32_16x16x32_bf16 v[76:79], v[236:239], v[196:199], v[76:79]\n"
    "v_mfma_f32_16x16x32_bf16 v[80:83], v[236:239], v[200:203], v[80:83]\n"
    "s_nop 5\n"
    "s_waitcnt lgkmcnt(8)\n"
    "v_mfma_f32_16x16x32_bf16 v[92:95], v[240:243], v[200:203], v[92:95]\n"
    "ds_read_b64_tr_b16 v[224:225], v247 offset:33024\n"
    "ds_read_b64_tr_b16 v[226:227], v247 offset:41216\n"
    "v_mfma_f32_16x16x32_bf16 v[88:91], v[240:243], v[196:199], v[88:91]\n"
    "v_mfma_f32_16x16x32_bf16 v[84:87], v[240:243], v[192:195], v[84:87]\n"
    "s_nop 5\n"
    "s_waitcnt lgkmcnt(7)\n"
    "v_mfma_f32_16x16x32_bf16 v[96:99], v[204:207], v[192:195], v[96:99]\n"
    "ds_read_b64_tr_b16 v[228:229], v248 offset:33024\n"
    "ds_read_b64_tr_b16 v[230:231], v248 offset:41216\n"
    "v_mfma_f32_16x16x32_bf16 v[100:103], v[204:207], v[196:199], v[100:103]\n"
    "v_mfma_f32_16x16x32_bf16 v[104:107], v[204:207], v[200:203], v[104:107]\n"
    "s_nop 5\n"
    "s_waitcnt lgkmcnt(6)\n"
    "v_mfma_f32_16x16x32_bf16 v[116:119], v[216:219], v[200:203], v[116:119]\n"
    "ds_read_b64_tr_b16 v[236:237], v249 offset:33024\n"
    "ds_read_b64_tr_b16 v[238:239], v249 offset:41216\n"
    "v_mfma_f32_16x16x32_bf16 v[112:115], v[216:219], v[196:199], v[112:115]\n"
    "v_mfma_f32_16x16x32_bf16 v[108:111], v[216:219], v[192:195], v[108:111]\n"
    "s_nop 5\n"
    "s_waitcnt lgkmcnt(6)\n"
    "v_mfma_f32_16x16x32_bf16 v[120:123], v[220:223], v[192:195], v[120:123]\n"
    "ds_read_b64_tr_b16 v[240:241], v250 offset:33024\n"
    "ds_read_b64_tr_b16 v[242:243], v250 offset:41216\n"
    "v_mfma_f32_16x16x32_bf16 v[124:127], v[220:223], v[196:199], v[124:127]\n"
    "v_mfma_f32_16x16x32_bf16 v[128:131], v[220:223], v[200:203], v[128:131]\n"
    "s_nop 5\n"
    "s_waitcnt lgkmcnt(6)\n"
    "v_mfma_f32_16x16x32_bf16 v[140:143], v[224:227], v[200:203], v[140:143]\n"
    "ds_read_b64_tr_b16 v[204:205], v251 offset:33024\n"
    "ds_read_b64_tr_b16 v[206:207], v251 offset:41216\n"
    "v_mfma_f32_16x16x32_bf16 v[136:139], v[224:227], v[196:199], v[136:139]\n"
    "v_mfma_f32_16x16x32_bf16 v[132:135], v[224:227], v[192:195], v[132:135]\n"
    "s_nop 5\n"
    "s_waitcnt lgkmcnt(6)\n"
    "v_mfma_f32_16x16x32_bf16 v[144:147], v[228:231], v[192:195], v[144:147]\n"
    "v_mfma_f32_16x16x32_bf16 v[148:151], v[228:231], v[196:199], v[148:151]\n"
    "v_mfma_f32_16x16x32_bf16 v[152:155], v[228:231], v[200:203], v[152:155]\n"
    "s_nop 5\n"
    "s_waitcnt lgkmcnt(0)\n"
    "s_barrier\n"
    "v_mfma_f32_16x16x32_bf16 v[164:167], v[236:239], v[200:203], v[164:167]\n"
    "ds_read_b64_tr_b16 v[216:217], v244 offset:49152\n"
    "ds_read_b64_tr_b16 v[218:219], v244 offset:57344\n"
    "v_mfma_f32_16x16x32_bf16 v[160:163], v[236:239], v[196:199], v[160:163]\n"
    "ds_read_b64_tr_b16 v[220:221], v245 offset:49152\n"
    "ds_read_b64_tr_b16 v[222:223], v245 offset:57344\n"
    "v_mfma_f32_16x16x32_bf16 v[156:159], v[236:239], v[192:195], v[156:159]\n"
    "s_nop 5\n"
    "v_mfma_f32_16x16x32_bf16 v[168:171], v[240:243], v[192:195], v[168:171]\n"
    "ds_read_b64_tr_b16 v[224:225], v246 offset:49152\n"
    "ds_read_b64_tr_b16 v[226:227], v246 offset:57344\n"
    "v_mfma_f32_16x16x32_bf16 v[172:175], v[240:243], v[196:199], v[172:175]\n"
    "v_mfma_f32_16x16x32_bf16 v[176:179], v[240:243], v[200:203], v[176:179]\n"
    "s_nop 5\n"
    "v_mfma_f32_16x16x32_bf16 v[188:191], v[204:207], v[200:203], v[188:191]\n"
    "ds_read_b64_tr_b16 v[228:229], v247 offset:49152\n"
    "ds_read_b64_tr_b16 v[230:231], v247 offset:57344\n"
    "v_mfma_f32_16x16x32_bf16 v[184:187], v[204:207], v[196:199], v[184:187]\n"
    "v_mfma_f32_16x16x32_bf16 v[180:183], v[204:207], v[192:195], v[180:183]\n"
    "s_nop 5\n"
    "s_waitcnt lgkmcnt(6)\n"
    "v_mfma_f32_16x16x32_bf16 v[0:3], v[216:219], v[232:235], v[0:3]\n"
    "ds_read_b64_tr_b16 v[236:237], v248 offset:49152\n"
    "ds_read_b64_tr_b16 v[238:239], v248 offset:57344\n"
    "v_mfma_f32_16x16x32_bf16 v[4:7], v[216:219], v[208:211], v[4:7]\n"
    "v_mfma_f32_16x16x32_bf16 v[8:11], v[216:219], v[212:215], v[8:11]\n"
    "s_nop 3\n"
    "s_waitcnt lgkmcnt(6)\n"
    "v_mfma_f32_16x16x32_bf16 v[20:23], v[220:223], v[212:215], v[20:23]\n"
    "ds_read_b64_tr_b16 v[240:241], v249 offset:49152\n"
    "ds_read_b64_tr_b16 v[242:243], v249 offset:57344\n"
    "v_mfma_f32_16x16x32_bf16 v[16:19], v[220:223], v[208:211], v[16:19]\n"
    "v_mfma_f32_16x16x32_bf16 v[12:15], v[220:223], v[232:235], v[12:15]\n"
    "s_nop 3\n"
    "s_waitcnt lgkmcnt(6)\n"
    "v_mfma_f32_16x16x32_bf16 v[24:27], v[224:227], v[232:235], v[24:27]\n"
    "ds_read_b64_tr_b16 v[204:205], v250 offset:49152\n"
    "ds_read_b64_tr_b16 v[206:207], v250 offset:57344\n"
    "v_mfma_f32_16x16x32_bf16 v[28:31], v[224:227], v[208:211], v[28:31]\n"
    "v_mfma_f32_16x16x32_bf16 v[32:35], v[224:227], v[212:215], v[32:35]\n"
    "s_nop 3\n"
    "s_waitcnt lgkmcnt(6)\n"
    "v_mfma_f32_16x16x32_bf16 v[44:47], v[228:231], v[212:215], v[44:47]\n"
    "ds_read_b64_tr_b16 v[192:193], v251 offset:49152\n"
    "ds_read_b64_tr_b16 v[194:195], v251 offset:57344\n"
    "v_mfma_f32_16x16x32_bf16 v[40:43], v[228:231], v[208:211], v[40:43]\n"
    "v_mfma_f32_16x16x32_bf16 v[36:39], v[228:231], v[232:235], v[36:39]\n"
    "s_nop 3\n"
    "s_waitcnt lgkmcnt(6)\n"
    "v_mfma_f32_16x16x32_bf16 v[48:51], v[236:239], v[232:235], v[48:51]\n"
    "ds_read_b64_tr_b16 v[196:197], v244 offset:49408\n"
    "ds_read_b64_tr_b16 v[198:199], v244 offset:57600\n"
    "v_mfma_f32_16x16x32_bf16 v[52:55], v[236:239], v[208:211], v[52:55]\n"
    "v_mfma_f32_16x16x32_bf16 v[56:59], v[236:239], v[212:215], v[56:59]\n"
    "s_nop 3\n"
    "s_waitcnt lgkmcnt(6)\n"
    "v_mfma_f32_16x16x32_bf16 v[68:71], v[240:243], v[212:215], v[68:71]\n"
    "ds_read_b64_tr_b16 v[200:201], v245 offset:49408\n"
    "ds_read_b64_tr_b16 v[202:203], v245 offset:57600\n"
    "v_mfma_f32_16x16x32_bf16 v[64:67], v[240:243], v[208:211], v[64:67]\n"
    "v_mfma_f32_16x16x32_bf16 v[60:63], v[240:243], v[232:235], v[60:63]\n"
    "s_nop 3\n"
    "s_waitcnt lgkmcnt(6)\n"
    "v_mfma_f32_16x16x32_bf16 v[72:75], v[204:207], v[232:235], v[72:75]\n"
    "ds_read_b64_tr_b16 v[216:217], v246 offset:49408\n"
    "ds_read_b64_tr_b16 v[218:219], v246 offset:57600\n"
    "v_mfma_f32_16x16x32_bf16 v[76:79], v[204:207], v[208:211], v[76:79]\n"
    "v_mfma_f32_16x16x32_bf16 v[80:83], v[204:207], v[212:215], v[80:83]\n"
    "s_nop 3\n"
    "s_waitcnt lgkmcnt(6)\n"
    "v_mfma_f32_16x16x32_bf16 v[92:95], v[192:195], v[212:215], v[92:95]\n"
    "ds_read_b64_tr_b16 v[220:221], v247 offset:49408\n"
    "ds_read_b64_tr_b16 v[222:223], v247 offset:57600\n"
    "v_mfma_f32_16x16x32_bf16 v[88:91], v[192:195], v[208:211], v[88:91]\n"
    "v_mfma_f32_16x16x32_bf16 v[84:87], v[192:195], v[232:235], v[84:87]\n"
    "s_nop 3\n"
    "s_waitcnt lgkmcnt(6)\n"
    "v_mfma_f32_16x16x32_bf16 v[96:99], v[196:199], v[232:235], v[96:99]\n"
    "ds_read_b64_tr_b16 v[224:225], v248 offset:49408\n"
    "ds_read_b64_tr_b16 v[226:227], v248 offset:57600\n"
    "v_mfma_f32_16x16x32_bf16 v[100:103], v[196:199], v[208:211], v[100:103]\n"
    "v_mfma_f32_16x16x32_bf16 v[104:107], v[196:199], v[212:215], v[104:107]\n"
    "s_nop 3\n"
    "s_waitcnt lgkmcnt(6)\n"
    "v_mfma_f32_16x16x32_bf16 v[116:119], v[200:203], v[212:215], v[116:119]\n"
    "ds_read_b64_tr_b16 v[228:229], v249 offset:49408\n"
    "ds_read_b64_tr_b16 v[230:231], v249 offset:57600\n"
    "v_mfma_f32_16x16x32_bf16 v[112:115], v[200:203], v[208:211], v[112:115]\n"
    "v_mfma_f32_16x16x32_bf16 v[108:111], v[200:203], v[232:235], v[108:111]\n"
    "s_nop 3\n"
    "s_waitcnt lgkmcnt(6)\n"
    "v_mfma_f32_16x16x32_bf16 v[120:123], v[216:219], v[232:235], v[120:123]\n"
    "ds_read_b64_tr_b16 v[236:237], v250 offset:49408\n"
    "ds_read_b64_tr_b16 v[238:239], v250 offset:57600\n"
    "v_mfma_f32_16x16x32_bf16 v[124:127], v[216:219], v[208:211], v[124:127]\n"
    "v_mfma_f32_16x16x32_bf16 v[128:131], v[216:219], v[212:215], v[128:131]\n"
    "s_nop 3\n"
    "s_waitcnt lgkmcnt(6)\n"
    "v_mfma_f32_16x16x32_bf16 v[140:143], v[220:223], v[212:215], v[140:143]\n"
    "ds_read_b64_tr_b16 v[240:241], v251 offset:49408\n"
    "ds_read_b64_tr_b16 v[242:243], v251 offset:57600\n"
    "v_mfma_f32_16x16x32_bf16 v[136:139], v[220:223], v[208:211], v[136:139]\n"
    "v_mfma_f32_16x16x32_bf16 v[132:135], v[220:223], v[232:235], v[132:135]\n"
    "s_nop 3\n"
    "s_waitcnt lgkmcnt(6)\n"
    "v_mfma_f32_16x16x32_bf16 v[144:147], v[224:227], v[232:235], v[144:147]\n"
    "v_mfma_f32_16x16x32_bf16 v[148:151], v[224:227], v[208:211], v[148:151]\n"
    "v_mfma_f32_16x16x32_bf16 v[152:155], v[224:227], v[212:215], v[152:155]\n"
    "s_nop 3\n"
    "s_waitcnt lgkmcnt(0)\n"
    "s_barrier\n"
    "v_mfma_f32_16x16x32_bf16 v[164:167], v[228:231], v[212:215], v[164:167]\n"
    "ds_read_b64_tr_b16 v[204:205], v244 offset:0\n"
    "ds_read_b64_tr_b16 v[206:207], v244 offset:8192\n"
    "v_mfma_f32_16x16x32_bf16 v[160:163], v[228:231], v[208:211], v[160:163]\n"
    "ds_read_b128 v[192:195], v252 offset:0\n"
    "ds_read_b128 v[196:199], v252 offset:2048\n"
    "v_mfma_f32_16x16x32_bf16 v[156:159], v[228:231], v[232:235], v[156:159]\n"
    "ds_read_b128 v[200:203], v252 offset:4096\n"
    "ds_read_b64_tr_b16 v[216:217], v245 offset:0\n"
    "s_nop 3\n"
    "v_mfma_f32_16x16x32_bf16 v[168:171], v[236:239], v[232:235], v[168:171]\n"
    "ds_read_b64_tr_b16 v[218:219], v245 offset:8192\n"
    "ds_read_b64_tr_b16 v[220:221], v246 offset:0\n"
    "v_mfma_f32_16x16x32_bf16 v[172:175], v[236:239], v[208:211], v[172:175]\n"
    "ds_read_b64_tr_b16 v[222:223], v246 offset:8192\n"
    "v_mfma_f32_16x16x32_bf16 v[176:179], v[236:239], v[212:215], v[176:179]\n"
    "s_nop 3\n"
    "v_mfma_f32_16x16x32_bf16 v[188:191], v[240:243], v[212:215], v[188:191]\n"
    "ds_read_b64_tr_b16 v[224:225], v247 offset:0\n"
    "ds_read_b64_tr_b16 v[226:227], v247 offset:8192\n"
    "v_mfma_f32_16x16x32_bf16 v[184:187], v[240:243], v[208:211], v[184:187]\n"
    "v_mfma_f32_16x16x32_bf16 v[180:183], v[240:243], v[232:235], v[180:183]\n"
    "s_nop 3\n"
    "s_waitcnt lgkmcnt(8)\n"
    "v_mfma_f32_16x16x32_bf16 v[0:3], v[204:207], v[192:195], v[0:3]\n"
    "ds_read_b64_tr_b16 v[228:229], v248 offset:0\n"
    "ds_read_b64_tr_b16 v[230:231], v248 offset:8192\n"
    "s_waitcnt lgkmcnt(9)\n"
    "v_mfma_f32_16x16x32_bf16 v[4:7], v[204:207], v[196:199], v[4:7]\n"
    "s_waitcnt lgkmcnt(8)\n"
    "v_mfma_f32_16x16x32_bf16 v[8:11], v[204:207], v[200:203], v[8:11]\n"
    "s_nop 3\n"
    "s_waitcnt lgkmcnt(6)\n"
    "v_mfma_f32_16x16x32_bf16 v[20:23], v[216:219], v[200:203], v[20:23]\n"
    "ds_read_b64_tr_b16 v[236:237], v249 offset:0\n"
    "ds_read_b64_tr_b16 v[238:239], v249 offset:8192\n"
    "v_mfma_f32_16x16x32_bf16 v[16:19], v[216:219], v[196:199], v[16:19]\n"
    "v_mfma_f32_16x16x32_bf16 v[12:15], v[216:219], v[192:195], v[12:15]\n"
    "s_nop 3\n"
    "s_waitcnt lgkmcnt(6)\n"
    "v_mfma_f32_16x16x32_bf16 v[24:27], v[220:223], v[192:195], v[24:27]\n"
    "ds_read_b64_tr_b16 v[240:241], v250 offset:0\n"
    "ds_read_b64_tr_b16 v[242:243], v250 offset:8192\n"
    "v_mfma_f32_16x16x32_bf16 v[28:31], v[220:223], v[196:199], v[28:31]\n"
    "ds_read_b128 v[232:235], v253 offset:0\n"
    "v_mfma_f32_16x16x32_bf16 v[32:35], v[220:223], v[200:203], v[32:35]\n"
    "s_nop 3\n"
    "s_waitcnt lgkmcnt(7)\n"
    "v_mfma_f32_16x16x32_bf16 v[44:47], v[224:227], v[200:203], v[44:47]\n"
    "ds_read_b64_tr_b16 v[208:209], v251 offset:0\n"
    "ds_read_b64_tr_b16 v[210:211], v251 offset:8192\n"
    "v_mfma_f32_16x16x32_bf16 v[40:43], v[224:227], v[196:199], v[40:43]\n"
    "ds_read_b128 v[212:215], v253 offset:2048\n"
    "v_mfma_f32_16x16x32_bf16 v[36:39], v[224:227], v[192:195], v[36:39]\n"
    "s_nop 3\n"
    "s_waitcnt lgkmcnt(8)\n"
    "v_mfma_f32_16x16x32_bf16 v[48:51], v[228:231], v[192:195], v[48:51]\n"
    "ds_read_b64_tr_b16 v[204:205], v244 offset:256\n"
    "ds_read_b64_tr_b16 v[206:207], v244 offset:8448\n"
    "v_mfma_f32_16x16x32_bf16 v[52:55], v[228:231], v[196:199], v[52:55]\n"
    "ds_read_b128 v[216:219], v253 offset:4096\n"
    "v_mfma_f32_16x16x32_bf16 v[56:59], v[228:231], v[200:203], v[56:59]\n"
    "s_nop 3\n"
    "s_waitcnt lgkmcnt(9)\n"
    "v_mfma_f32_16x16x32_bf16 v[68:71], v[236:239], v[200:203], v[68:71]\n"
    "ds_read_b64_tr_b16 v[220:221], v245 offset:256\n"
    "ds_read_b64_tr_b16 v[222:223], v245 offset:8448\n"
    "v_mfma_f32_16x16x32_bf16 v[64:67], v[236:239], v[196:199], v[64:67]\n"
    "v_mfma_f32_16x16x32_bf16 v[60:63], v[236:239], v[192:195], v[60:63]\n"
    "s_nop 3\n"
    "s_waitcnt lgkmcnt(9)\n"
    "v_mfma_f32_16x16x32_bf16 v[72:75], v[240:243], v[192:195], v[72:75]\n"
    "ds_read_b64_tr_b16 v[224:225], v246 offset:256\n"
    "ds_read_b64_tr_b16 v[226:227], v246 offset:8448\n"
    "v_mfma_f32_16x16x32_bf16 v[76:79], v[240:243], v[196:199], v[76:79]\n"
    "v_mfma_f32_16x16x32_bf16 v[80:83], v[240:243], v[200:203], v[80:83]\n"
    "s_nop 3\n"
    "s_waitcnt lgkmcnt(8)\n"
    "v_mfma_f32_16x16x32_bf16 v[92:95], v[208:211], v[200:203], v[92:95]\n"
    "ds_read_b64_tr_b16 v[228:229], v247 offset:256\n"
    "ds_read_b64_tr_b16 v[230:231], v247 offset:8448\n"
    "v_mfma_f32_16x16x32_bf16 v[88:91], v[208:211], v[196:199], v[88:91]\n"
    "v_mfma_f32_16x16x32_bf16 v[84:87], v[208:211], v[192:195], v[84:87]\n"
    "s_nop 3\n"
    "s_waitcnt lgkmcnt(7)\n"
    "v_mfma_f32_16x16x32_bf16 v[96:99], v[204:207], v[192:195], v[96:99]\n"
    "ds_read_b64_tr_b16 v[236:237], v248 offset:256\n"
    "ds_read_b64_tr_b16 v[238:239], v248 offset:8448\n"
    "v_mfma_f32_16x16x32_bf16 v[100:103], v[204:207], v[196:199], v[100:103]\n"
    "v_mfma_f32_16x16x32_bf16 v[104:107], v[204:207], v[200:203], v[104:107]\n"
    "s_nop 3\n"
    "s_waitcnt lgkmcnt(6)\n"
    "v_mfma_f32_16x16x32_bf16 v[116:119], v[220:223], v[200:203], v[116:119]\n"
    "ds_read_b64_tr_b16 v[240:241], v249 offset:256\n"
    "ds_read_b64_tr_b16 v[242:243], v249 offset:8448\n"
    "v_mfma_f32_16x16x32_bf16 v[112:115], v[220:223], v[196:199], v[112:115]\n"
    "v_mfma_f32_16x16x32_bf16 v[108:111], v[220:223], v[192:195], v[108:111]\n"
    "s_nop 3\n"
    "s_waitcnt lgkmcnt(6)\n"
    "v_mfma_f32_16x16x32_bf16 v[120:123], v[224:227], v[192:195], v[120:123]\n"
    "ds_read_b64_tr_b16 v[208:209], v250 offset:256\n"
    "ds_read_b64_tr_b16 v[210:211], v250 offset:8448\n"
    "v_mfma_f32_16x16x32_bf16 v[124:127], v[224:227], v[196:199], v[124:127]\n"
    "v_mfma_f32_16x16x32_bf16 v[128:131], v[224:227], v[200:203], v[128:131]\n"
    "s_nop 3\n"
    "s_waitcnt lgkmcnt(6)\n"
    "v_mfma_f32_16x16x32_bf16 v[140:143], v[228:231], v[200:203], v[140:143]\n"
    "ds_read_b64_tr_b16 v[204:205], v251 offset:256\n"
    "ds_read_b64_tr_b16 v[206:207], v251 offset:8448\n"
    "v_mfma_f32_16x16x32_bf16 v[136:139], v[228:231], v[196:199], v[136:139]\n"
    "v_mfma_f32_16x16x32_bf16 v[132:135], v[228:231], v[192:195], v[132:135]\n"
    "s_nop 3\n"
    "s_waitcnt lgkmcnt(6)\n"
    "v_mfma_f32_16x16x32_bf16 v[144:147], v[236:239], v[192:195], v[144:147]\n"
    "v_mfma_f32_16x16x32_bf16 v[148:151], v[236:239], v[196:199], v[148:151]\n"
    "v_mfma_f32_16x16x32_bf16 v[152:155], v[236:239], v[200:203], v[152:155]\n"
    "s_nop 3\n"
    "s_waitcnt lgkmcnt(0)\n"
    "s_barrier\n"
    "v_mfma_f32_16x16x32_bf16 v[164:167], v[240:243], v[200:203], v[164:167]\n"
    "ds_read_b64_tr_b16 v[220:221], v244 offset:16384\n"
    "ds_read_b64_tr_b16 v[222:223], v244 offset:24576\n"
    "v_mfma_f32_16x16x32_bf16 v[160:163], v[240:243], v[196:199], v[160:163]\n"
    "ds_read_b64_tr_b16 v[224:225], v245 offset:16384\n"
    "ds_read_b64_tr_b16 v[226:227], v245 offset:24576\n"
    "v_mfma_f32_16x16x32_bf16 v[156:159], v[240:243], v[192:195], v[156:159]\n"
    "s_nop 3\n"
    "v_mfma_f32_16x16x32_bf16 v[168:171], v[208:211], v[192:195], v[168:171]\n"
    "ds_read_b64_tr_b16 v[228:229], v246 offset:16384\n"
    "ds_read_b64_tr_b16 v[230:231], v246 offset:24576\n"
    "v_mfma_f32_16x16x32_bf16 v[172:175], v[208:211], v[196:199], v[172:175]\n"
    "v_mfma_f32_16x16x32_bf16 v[176:179], v[208:211], v[200:203], v[176:179]\n"
    "s_nop 3\n"
    "v_mfma_f32_16x16x32_bf16 v[188:191], v[204:207], v[200:203], v[188:191]\n"
    "ds_read_b64_tr_b16 v[236:237], v247 offset:16384\n"
    "ds_read_b64_tr_b16 v[238:239], v247 offset:24576\n"
    "v_mfma_f32_16x16x32_bf16 v[184:187], v[204:207], v[196:199], v[184:187]\n"
    "v_mfma_f32_16x16x32_bf16 v[180:183], v[204:207], v[192:195], v[180:183]\n"
    "s_nop 3\n"
    "s_waitcnt lgkmcnt(6)\n"
    "v_mfma_f32_16x16x32_bf16 v[0:3], v[220:223], v[232:235], v[0:3]\n"
    "ds_read_b64_tr_b16 v[240:241], v248 offset:16384\n"
    "ds_read_b64_tr_b16 v[242:243], v248 offset:24576\n"
    "v_mfma_f32_16x16x32_bf16 v[4:7], v[220:223], v[212:215], v[4:7]\n"
    "v_mfma_f32_16x16x32_bf16 v[8:11], v[220:223], v[216:219], v[8:11]\n"
    "s_nop 2\n"
    "s_waitcnt lgkmcnt(6)\n"
    "v_mfma_f32_16x16x32_bf16 v[20:23], v[224:227], v[216:219], v[20:23]\n"
    "ds_read_b64_tr_b16 v[208:209], v249 offset:16384\n"
    "ds_read_b64_tr_b16 v[210:211], v249 offset:24576\n"
    "v_mfma_f32_16x16x32_bf16 v[16:19], v[224:227], v[212:215], v[16:19]\n"
    "v_mfma_f32_16x16x32_bf16 v[12:15], v[224:227], v[232:235], v[12:15]\n"
    "s_nop 2\n"
    "s_waitcnt lgkmcnt(6)\n"
    "v_mfma_f32_16x16x32_bf16 v[24:27], v[228:231], v[232:235], v[24:27]\n"
    "ds_read_b64_tr_b16 v[204:205], v250 offset:16384\n"
    "ds_read_b64_tr_b16 v[206:207], v250 offset:24576\n"
    "v_mfma_f32_16x16x32_bf16 v[28:31], v[228:231], v[212:215], v[28:31]\n"
    "v_mfma_f32_16x16x32_bf16 v[32:35], v[228:231], v[216:219], v[32:35]\n"
    "s_nop 2\n"
    "s_waitcnt lgkmcnt(6)\n"
    "v_mfma_f32_16x16x32_bf16 v[44:47], v[236:239], v[216:219], v[44:47]\n"
    "ds_read_b64_tr_b16 v[192:193], v251 offset:16384\n"
    "ds_read_b64_tr_b16 v[194:195], v251 offset:24576\n"
    "v_mfma_f32_16x16x32_bf16 v[40:43], v[236:239], v[212:215], v[40:43]\n"
    "v_mfma_f32_16x16x32_bf16 v[36:39], v[236:239], v[232:235], v[36:39]\n"
    "s_nop 2\n"
    "s_waitcnt lgkmcnt(6)\n"
    "v_mfma_f32_16x16x32_bf16 v[48:51], v[240:243], v[232:235], v[48:51]\n"
    "ds_read_b64_tr_b16 v[196:197], v244 offset:16640\n"
    "ds_read_b64_tr_b16 v[198:199], v244 offset:24832\n"
    "v_mfma_f32_16x16x32_bf16 v[52:55], v[240:243], v[212:215], v[52:55]\n"
    "v_mfma_f32_16x16x32_bf16 v[56:59], v[240:243], v[216:219], v[56:59]\n"
    "s_nop 2\n"
    "s_waitcnt lgkmcnt(6)\n"
    "v_mfma_f32_16x16x32_bf16 v[68:71], v[208:211], v[216:219], v[68:71]\n"
    "ds_read_b64_tr_b16 v[200:201], v245 offset:16640\n"
    "ds_read_b64_tr_b16 v[202:203], v245 offset:24832\n"
    "v_mfma_f32_16x16x32_bf16 v[64:67], v[208:211], v[212:215], v[64:67]\n"
    "v_mfma_f32_16x16x32_bf16 v[60:63], v[208:211], v[232:235], v[60:63]\n"
    "s_nop 2\n"
    "s_waitcnt lgkmcnt(6)\n"
    "v_mfma_f32_16x16x32_bf16 v[72:75], v[204:207], v[232:235], v[72:75]\n"
    "ds_read_b64_tr_b16 v[220:221], v246 offset:16640\n"
    "ds_read_b64_tr_b16 v[222:223], v246 offset:24832\n"
    "v_mfma_f32_16x16x32_bf16 v[76:79], v[204:207], v[212:215], v[76:79]\n"
    "v_mfma_f32_16x16x32_bf16 v[80:83], v[204:207], v[216:219], v[80:83]\n"
    "s_nop 2\n"
    "s_waitcnt lgkmcnt(6)\n"
    "v_mfma_f32_16x16x32_bf16 v[92:95], v[192:195], v[216:219], v[92:95]\n"
    "ds_read_b64_tr_b16 v[224:225], v247 offset:16640\n"
    "ds_read_b64_tr_b16 v[226:227], v247 offset:24832\n"
    "v_mfma_f32_16x16x32_bf16 v[88:91], v[192:195], v[212:215], v[88:91]\n"
    "v_mfma_f32_16x16x32_bf16 v[84:87], v[192:195], v[232:235], v[84:87]\n"
    "s_nop 2\n"
    "s_waitcnt lgkmcnt(6)\n"
    "v_mfma_f32_16x16x32_bf16 v[96:99], v[196:199], v[232:235], v[96:99]\n"
    "ds_read_b64_tr_b16 v[228:229], v248 offset:16640\n"
    "ds_read_b64_tr_b16 v[230:231], v248 offset:24832\n"
    "v_mfma_f32_16x16x32_bf16 v[100:103], v[196:199], v[212:215], v[100:103]\n"
    "v_mfma_f32_16x16x32_bf16 v[104:107], v[196:199], v[216:219], v[104:107]\n"
    "s_nop 2\n"
    "s_waitcnt lgkmcnt(6)\n"
    "v_mfma_f32_16x16x32_bf16 v[116:119], v[200:203], v[216:219], v[116:119]\n"
    "ds_read_b64_tr_b16 v[236:237], v249 offset:16640\n"
    "ds_read_b64_tr_b16 v[238:239], v249 offset:24832\n"
    "v_mfma_f32_16x16x32_bf16 v[112:115], v[200:203], v[212:215], v[112:115]\n"
    "v_mfma_f32_16x16x32_bf16 v[108:111], v[200:203], v[232:235], v[108:111]\n"
    "s_nop 2\n"
    "s_waitcnt lgkmcnt(6)\n"
    "v_mfma_f32_16x16x32_bf16 v[120:123], v[220:223], v[232:235], v[120:123]\n"
    "ds_read_b64_tr_b16 v[240:241], v250 offset:16640\n"
    "ds_read_b64_tr_b16 v[242:243], v250 offset:24832\n"
    "v_mfma_f32_16x16x32_bf16 v[124:127], v[220:223], v[212:215], v[124:127]\n"
    "v_mfma_f32_16x16x32_bf16 v[128:131], v[220:223], v[216:219], v[128:131]\n"
    "s_nop 2\n"
    "s_waitcnt lgkmcnt(6)\n"
    "v_mfma_f32_16x16x32_bf16 v[140:143], v[224:227], v[216:219], v[140:143]\n"
    "ds_read_b64_tr_b16 v[208:209], v251 offset:16640\n"
    "ds_read_b64_tr_b16 v[210:211], v251 offset:24832\n"
    "v_mfma_f32_16x16x32_bf16 v[136:139], v[224:227], v[212:215], v[136:139]\n"
    "v_mfma_f32_16x16x32_bf16 v[132:135], v[224:227], v[232:235], v[132:135]\n"
    "s_nop 2\n"
    "s_waitcnt lgkmcnt(6)\n"
    "v_mfma_f32_16x16x32_bf16 v[144:147], v[228:231], v[232:235], v[144:147]\n"
    "v_mfma_f32_16x16x32_bf16 v[148:151], v[228:231], v[212:215], v[148:151]\n"
    "v_mfma_f32_16x16x32_bf16 v[152:155], v[228:231], v[216:219], v[152:155]\n"
    "s_nop 2\n"
    "s_waitcnt lgkmcnt(0)\n"
    "s_barrier\n"
    "v_mfma_f32_16x16x32_bf16 v[164:167], v[236:239], v[216:219], v[164:167]\n"
    "ds_read_b64_tr_b16 v[204:205], v244 offset:32768\n"
    "ds_read_b64_tr_b16 v[206:207], v244 offset:40960\n"
    "v_mfma_f32_16x16x32_bf16 v[160:163], v[236:239], v[212:215], v[160:163]\n"
    "ds_read_b128 v[192:195], v252 offset:6144\n"
    "ds_read_b128 v[196:199], v252 offset:8192\n"
    "v_mfma_f32_16x16x32_bf16 v[156:159], v[236:239], v[232:235], v[156:159]\n"
    "ds_read_b128 v[200:203], v252 offset:10240\n"
    "ds_read_b64_tr_b16 v[220:221], v245 offset:32768\n"
    "s_nop 2\n"
    "v_mfma_f32_16x16x32_bf16 v[168:171], v[240:243], v[232:235], v[168:171]\n"
    "ds_read_b64_tr_b16 v[222:223], v245 offset:40960\n"
    "ds_read_b64_tr_b16 v[224:225], v246 offset:32768\n"
    "v_mfma_f32_16x16x32_bf16 v[172:175], v[240:243], v[212:215], v[172:175]\n"
    "ds_read_b64_tr_b16 v[226:227], v246 offset:40960\n"
    "v_mfma_f32_16x16x32_bf16 v[176:179], v[240:243], v[216:219], v[176:179]\n"
    "s_nop 2\n"
    "v_mfma_f32_16x16x32_bf16 v[188:191], v[208:211], v[216:219], v[188:191]\n"
    "ds_read_b64_tr_b16 v[228:229], v247 offset:32768\n"
    "ds_read_b64_tr_b16 v[230:231], v247 offset:40960\n"
    "v_mfma_f32_16x16x32_bf16 v[184:187], v[208:211], v[212:215], v[184:187]\n"
    "v_mfma_f32_16x16x32_bf16 v[180:183], v[208:211], v[232:235], v[180:183]\n"
    "s_nop 2\n"
    "s_waitcnt lgkmcnt(8)\n"
    "v_mfma_f32_16x16x32_bf16 v[0:3], v[204:207], v[192:195], v[0:3]\n"
    "ds_read_b64_tr_b16 v[236:237], v248 offset:32768\n"
    "ds_read_b64_tr_b16 v[238:239], v248 offset:40960\n"
    "s_waitcnt lgkmcnt(9)\n"
    "v_mfma_f32_16x16x32_bf16 v[4:7], v[204:207], v[196:199], v[4:7]\n"
    "s_waitcnt lgkmcnt(8)\n"
    "v_mfma_f32_16x16x32_bf16 v[8:11], v[204:207], v[200:203], v[8:11]\n"
    "s_nop 2\n"
    "s_waitcnt lgkmcnt(6)\n"
    "v_mfma_f32_16x16x32_bf16 v[20:23], v[220:223], v[200:203], v[20:23]\n"
    "ds_read_b64_tr_b16 v[240:241], v249 offset:32768\n"
    "ds_read_b64_tr_b16 v[242:243], v249 offset:40960\n"
    "v_mfma_f32_16x16x32_bf16 v[16:19], v[220:223], v[196:199], v[16:19]\n"
    "v_mfma_f32_16x16x32_bf16 v[12:15], v[220:223], v[192:195], v[12:15]\n"
    "s_nop 2\n"
    "s_waitcnt lgkmcnt(6)\n"
    "v_mfma_f32_16x16x32_bf16 v[24:27], v[224:227], v[192:195], v[24:27]\n"
    "ds_read_b64_tr_b16 v[208:209], v250 offset:32768\n"
    "ds_read_b64_tr_b16 v[210:211], v250 offset:40960\n"
    "v_mfma_f32_16x16x32_bf16 v[28:31], v[224:227], v[196:199], v[28:31]\n"
    "ds_read_b128 v[232:235], v253 offset:6144\n"
    "v_mfma_f32_16x16x32_bf16 v[32:35], v[224:227], v[200:203], v[32:35]\n"
    "s_nop 2\n"
    "s_waitcnt lgkmcnt(7)\n"
    "v_mfma_f32_16x16x32_bf16 v[44:47], v[228:231], v[200:203], v[44:47]\n"
    "ds_read_b64_tr_b16 v[212:213], v251 offset:32768\n"
    "ds_read_b64_tr_b16 v[214:215], v251 offset:40960\n"
    "v_mfma_f32_16x16x32_bf16 v[40:43], v[228:231], v[196:199], v[40:43]\n"
    "ds_read_b128 v[216:219], v253 offset:8192\n"
    "v_mfma_f32_16x16x32_bf16 v[36:39], v[228:231], v[192:195], v[36:39]\n"
    "s_nop 2\n"
    "s_waitcnt lgkmcnt(8)\n"
    "v_mfma_f32_16x16x32_bf16 v[48:51], v[236:239], v[192:195], v[48:51]\n"
    "ds_read_b64_tr_b16 v[204:205], v244 offset:33024\n"
    "ds_read_b64_tr_b16 v[206:207], v244 offset:41216\n"
    "v_mfma_f32_16x16x32_bf16 v[52:55], v[236:239], v[196:199], v[52:55]\n"
    "ds_read_b128 v[220:223], v253 offset:10240\n"
    "v_mfma_f32_16x16x32_bf16 v[56:59], v[236:239], v[200:203], v[56:59]\n"
    "s_nop 2\n"
    "s_waitcnt lgkmcnt(9)\n"
    "v_mfma_f32_16x16x32_bf16 v[68:71], v[240:243], v[200:203], v[68:71]\n"
    "ds_read_b64_tr_b16 v[224:225], v245 offset:33024\n"
    "ds_read_b64_tr_b16 v[226:227], v245 offset:41216\n"
    "v_mfma_f32_16x16x32_bf16 v[64:67], v[240:243], v[196:199], v[64:67]\n"
    "v_mfma_f32_16x16x32_bf16 v[60:63], v[240:243], v[192:195], v[60:63]\n"
    "s_nop 2\n"
    "s_waitcnt lgkmcnt(9)\n"
    "v_mfma_f32_16x16x32_bf16 v[72:75], v[208:211], v[192:195], v[72:75]\n"
    "ds_read_b64_tr_b16 v[228:229], v246 offset:33024\n"
    "ds_read_b64_tr_b16 v[230:231], v246 offset:41216\n"
    "v_mfma_f32_16x16x32_bf16 v[76:79], v[208:211], v[196:199], v[76:79]\n"
    "v_mfma_f32_16x16x32_bf16 v[80:83], v[208:211], v[200:203], v[80:83]\n"
    "s_nop 2\n"
    "s_waitcnt lgkmcnt(8)\n"
    "v_mfma_f32_16x16x32_bf16 v[92:95], v[212:215], v[200:203], v[92:95]\n"
    "ds_read_b64_tr_b16 v[236:237], v247 offset:33024\n"
    "ds_read_b64_tr_b16 v[238:239], v247 offset:41216\n"
    "v_mfma_f32_16x16x32_bf16 v[88:91], v[212:215], v[196:199], v[88:91]\n"
    "v_mfma_f32_16x16x32_bf16 v[84:87], v[212:215], v[192:195], v[84:87]\n"
    "s_nop 2\n"
    "s_waitcnt lgkmcnt(7)\n"
    "v_mfma_f32_16x16x32_bf16 v[96:99], v[204:207], v[192:195], v[96:99]\n"
    "ds_read_b64_tr_b16 v[240:241], v248 offset:33024\n"
    "ds_read_b64_tr_b16 v[242:243], v248 offset:41216\n"
    "v_mfma_f32_16x16x32_bf16 v[100:103], v[204:207], v[196:199], v[100:103]\n"
    "v_mfma_f32_16x16x32_bf16 v[104:107], v[204:207], v[200:203], v[104:107]\n"
    "s_nop 2\n"
    "s_waitcnt lgkmcnt(6)\n"
    "v_mfma_f32_16x16x32_bf16 v[116:119], v[224:227], v[200:203], v[116:119]\n"
    "ds_read_b64_tr_b16 v[208:209], v249 offset:33024\n"
    "ds_read_b64_tr_b16 v[210:211], v249 offset:41216\n"
    "v_mfma_f32_16x16x32_bf16 v[112:115], v[224:227], v[196:199], v[112:115]\n"
    "v_mfma_f32_16x16x32_bf16 v[108:111], v[224:227], v[192:195], v[108:111]\n"
    "s_nop 2\n"
    "s_waitcnt lgkmcnt(6)\n"
    "v_mfma_f32_16x16x32_bf16 v[120:123], v[228:231], v[192:195], v[120:123]\n"
    "ds_read_b64_tr_b16 v[212:213], v250 offset:33024\n"
    "ds_read_b64_tr_b16 v[214:215], v250 offset:41216\n"
    "v_mfma_f32_16x16x32_bf16 v[124:127], v[228:231], v[196:199], v[124:127]\n"
    "v_mfma_f32_16x16x32_bf16 v[128:131], v[228:231], v[200:203], v[128:131]\n"
    "s_nop 2\n"
    "s_waitcnt lgkmcnt(6)\n"
    "v_mfma_f32_16x16x32_bf16 v[140:143], v[236:239], v[200:203], v[140:143]\n"
    "ds_read_b64_tr_b16 v[204:205], v251 offset:33024\n"
    "ds_read_b64_tr_b16 v[206:207], v251 offset:41216\n"
    "v_mfma_f32_16x16x32_bf16 v[136:139], v[236:239], v[196:199], v[136:139]\n"
    "v_mfma_f32_16x16x32_bf16 v[132:135], v[236:239], v[192:195], v[132:135]\n"
    "s_nop 2\n"
    "s_waitcnt lgkmcnt(6)\n"
    "v_mfma_f32_16x16x32_bf16 v[144:147], v[240:243], v[192:195], v[144:147]\n"
    "v_mfma_f32_16x16x32_bf16 v[148:151], v[240:243], v[196:199], v[148:151]\n"
    "v_mfma_f32_16x16x32_bf16 v[152:155], v[240:243], v[200:203], v[152:155]\n"
    "s_nop 2\n"
    "s_waitcnt lgkmcnt(0)\n"
    "s_barrier\n"
    "v_mfma_f32_16x16x32_bf16 v[164:167], v[208:211], v[200:203], v[164:167]\n"
    "ds_read_b64_tr_b16 v[224:225], v244 offset:49152\n"
    "ds_read_b64_tr_b16 v[226:227], v244 offset:57344\n"
    "v_mfma_f32_16x16x32_bf16 v[160:163], v[208:211], v[196:199], v[160:163]\n"
    "ds_read_b64_tr_b16 v[228:229], v245 offset:49152\n"
    "ds_read_b64_tr_b16 v[230:231], v245 offset:57344\n"
    "v_mfma_f32_16x16x32_bf16 v[156:159], v[208:211], v[192:195], v[156:159]\n"
    "s_nop 2\n"
    "v_mfma_f32_16x16x32_bf16 v[168:171], v[212:215], v[192:195], v[168:171]\n"
    "ds_read_b64_tr_b16 v[236:237], v246 offset:49152\n"
    "ds_read_b64_tr_b16 v[238:239], v246 offset:57344\n"
    "v_mfma_f32_16x16x32_bf16 v[172:175], v[212:215], v[196:199], v[172:175]\n"
    "v_mfma_f32_16x16x32_bf16 v[176:179], v[212:215], v[200:203], v[176:179]\n"
    "s_nop 2\n"
    "v_mfma_f32_16x16x32_bf16 v[188:191], v[204:207], v[200:203], v[188:191]\n"
    "ds_read_b64_tr_b16 v[240:241], v247 offset:49152\n"
    "ds_read_b64_tr_b16 v[242:243], v247 offset:57344\n"
    "v_mfma_f32_16x16x32_bf16 v[184:187], v[204:207], v[196:199], v[184:187]\n"
    "v_mfma_f32_16x16x32_bf16 v[180:183], v[204:207], v[192:195], v[180:183]\n"
    "s_nop 2\n"
    "s_waitcnt lgkmcnt(6)\n"
    "v_mfma_f32_16x16x32_bf16 v[0:3], v[224:227], v[232:235], v[0:3]\n"
    "ds_read_b64_tr_b16 v[208:209], v248 offset:49152\n"
    "ds_read_b64_tr_b16 v[210:211], v248 offset:57344\n"
    "v_mfma_f32_16x16x32_bf16 v[4:7], v[224:227], v[216:219], v[4:7]\n"
    "v_mfma_f32_16x16x32_bf16 v[8:11], v[224:227], v[220:223], v[8:11]\n"
    "s_waitcnt lgkmcnt(6)\n"
    "v_mfma_f32_16x16x32_bf16 v[20:23], v[228:231], v[220:223], v[20:23]\n"
    "ds_read_b64_tr_b16 v[212:213], v249 offset:49152\n"
    "ds_read_b64_tr_b16 v[214:215], v249 offset:57344\n"
    "v_mfma_f32_16x16x32_bf16 v[16:19], v[228:231], v[216:219], v[16:19]\n"
    "v_mfma_f32_16x16x32_bf16 v[12:15], v[228:231], v[232:235], v[12:15]\n"
    "s_waitcnt lgkmcnt(6)\n"
    "v_mfma_f32_16x16x32_bf16 v[24:27], v[236:239], v[232:235], v[24:27]\n"
    "ds_read_b64_tr_b16 v[204:205], v250 offset:49152\n"
    "ds_read_b64_tr_b16 v[206:207], v250 offset:57344\n"
    "v_mfma_f32_16x16x32_bf16 v[28:31], v[236:239], v[216:219], v[28:31]\n"
    "v_mfma_f32_16x16x32_bf16 v[32:35], v[236:239], v[220:223], v[32:35]\n"
    "s_waitcnt lgkmcnt(6)\n"
    "v_mfma_f32_16x16x32_bf16 v[44:47], v[240:243], v[220:223], v[44:47]\n"
    "ds_read_b64_tr_b16 v[192:193], v251 offset:49152\n"
    "ds_read_b64_tr_b16 v[194:195], v251 offset:57344\n"
    "v_mfma_f32_16x16x32_bf16 v[40:43], v[240:243], v[216:219], v[40:43]\n"
    "v_mfma_f32_16x16x32_bf16 v[36:39], v[240:243], v[232:235], v[36:39]\n"
    "s_waitcnt lgkmcnt(6)\n"
    "v_mfma_f32_16x16x32_bf16 v[48:51], v[208:211], v[232:235], v[48:51]\n"
    "ds_read_b64_tr_b16 v[196:197], v244 offset:49408\n"
    "ds_read_b64_tr_b16 v[198:199], v244 offset:57600\n"
    "v_mfma_f32_16x16x32_bf16 v[52:55], v[208:211], v[216:219], v[52:55]\n"
    "v_mfma_f32_16x16x32_bf16 v[56:59], v[208:211], v[220:223], v[56:59]\n"
    "s_waitcnt lgkmcnt(6)\n"
    "v_mfma_f32_16x16x32_bf16 v[68:71], v[212:215], v[220:223], v[68:71]\n"
    "ds_read_b64_tr_b16 v[200:201], v245 offset:49408\n"
    "ds_read_b64_tr_b16 v[202:203], v245 offset:57600\n"
    "v_mfma_f32_16x16x32_bf16 v[64:67], v[212:215], v[216:219], v[64:67]\n"
    "v_mfma_f32_16x16x32_bf16 v[60:63], v[212:215], v[232:235], v[60:63]\n"
    "s_waitcnt lgkmcnt(6)\n"
    "v_mfma_f32_16x16x32_bf16 v[72:75], v[204:207], v[232:235], v[72:75]\n"
    "ds_read_b64_tr_b16 v[224:225], v246 offset:49408\n"
    "ds_read_b64_tr_b16 v[226:227], v246 offset:57600\n"
    "v_mfma_f32_16x16x32_bf16 v[76:79], v[204:207], v[216:219], v[76:79]\n"
    "v_mfma_f32_16x16x32_bf16 v[80:83], v[204:207], v[220:223], v[80:83]\n"
    "s_waitcnt lgkmcnt(6)\n"
    "v_mfma_f32_16x16x32_bf16 v[92:95], v[192:195], v[220:223], v[92:95]\n"
    "ds_read_b64_tr_b16 v[228:229], v247 offset:49408\n"
    "ds_read_b64_tr_b16 v[230:231], v247 offset:57600\n"
    "v_mfma_f32_16x16x32_bf16 v[88:91], v[192:195], v[216:219], v[88:91]\n"
    "v_mfma_f32_16x16x32_bf16 v[84:87], v[192:195], v[232:235], v[84:87]\n"
    "s_waitcnt lgkmcnt(6)\n"
    "v_mfma_f32_16x16x32_bf16 v[96:99], v[196:199], v[232:235], v[96:99]\n"
    "ds_read_b64_tr_b16 v[236:237], v248 offset:49408\n"
    "ds_read_b64_tr_b16 v[238:239], v248 offset:57600\n"
    "v_mfma_f32_16x16x32_bf16 v[100:103], v[196:199], v[216:219], v[100:103]\n"
    "v_mfma_f32_16x16x32_bf16 v[104:107], v[196:199], v[220:223], v[104:107]\n"
    "s_waitcnt lgkmcnt(6)\n"
    "v_mfma_f32_16x16x32_bf16 v[116:119], v[200:203], v[220:223], v[116:119]\n"
    "ds_read_b64_tr_b16 v[240:241], v249 offset:49408\n"
    "ds_read_b64_tr_b16 v[242:243], v249 offset:57600\n"
    "v_mfma_f32_16x16x32_bf16 v[112:115], v[200:203], v[216:219], v[112:115]\n"
    "v_mfma_f32_16x16x32_bf16 v[108:111], v[200:203], v[232:235], v[108:111]\n"
    "s_waitcnt lgkmcnt(6)\n"
    "v_mfma_f32_16x16x32_bf16 v[120:123], v[224:227], v[232:235], v[120:123]\n"
    "ds_read_b64_tr_b16 v[208:209], v250 offset:49408\n"
    "ds_read_b64_tr_b16 v[210:211], v250 offset:57600\n"
    "v_mfma_f32_16x16x32_bf16 v[124:127], v[224:227], v[216:219], v[124:127]\n"
    "v_mfma_f32_16x16x32_bf16 v[128:131], v[224:227], v[220:223], v[128:131]\n"
    "s_waitcnt lgkmcnt(6)\n"
    "v_mfma_f32_16x16x32_bf16 v[140:143], v[228:231], v[220:223], v[140:143]\n"
    "ds_read_b64_tr_b16 v[212:213], v251 offset:49408\n"
    "ds_read_b64_tr_b16 v[214:215], v251 offset:57600\n"
    "v_mfma_f32_16x16x32_bf16 v[136:139], v[228:231], v[216:219], v[136:139]\n"
    "v_mfma_f32_16x16x32_bf16 v[132:135], v[228:231], v[232:235], v[132:135]\n"
    "s_waitcnt lgkmcnt(6)\n"
    "v_mfma_f32_16x16x32_bf16 v[144:147], v[236:239], v[232:235], v[144:147]\n"
    "v_mfma_f32_16x16x32_bf16 v[148:151], v[236:239], v[216:219], v[148:151]\n"
    "v_mfma_f32_16x16x32_bf16 v[152:155], v[236:239], v[220:223], v[152:155]\n"
    "s_waitcnt lgkmcnt(0)\n"
    "s_barrier\n"
    "v_mfma_f32_16x16x32_bf16 v[164:167], v[240:243], v[220:223], v[164:167]\n"
    "ds_read_b64_tr_b16 v[204:205], v244 offset:0\n"
    "ds_read_b64_tr_b16 v[206:207], v244 offset:8192\n"
    "v_mfma_f32_16x16x32_bf16 v[160:163], v[240:243], v[216:219], v[160:163]\n"
    "ds_read_b128 v[192:195], v252 offset:0\n"
    "ds_read_b128 v[196:199], v252 offset:2048\n"
    "v_mfma_f32_16x16x32_bf16 v[156:159], v[240:243], v[232:235], v[156:159]\n"
    "ds_read_b128 v[200:203], v252 offset:4096\n"
    "ds_read_b64_tr_b16 v[224:225], v245 offset:0\n"
    "v_mfma_f32_16x16x32_bf16 v[168:171], v[208:211], v[232:235], v[168:171]\n"
    "ds_read_b64_tr_b16 v[226:227], v245 offset:8192\n"
    "ds_read_b64_tr_b16 v[228:229], v246 offset:0\n"
    "v_mfma_f32_16x16x32_bf16 v[172:175], v[208:211], v[216:219], v[172:175]\n"
    "ds_read_b64_tr_b16 v[230:231], v246 offset:8192\n"
    "v_mfma_f32_16x16x32_bf16 v[176:179], v[208:211], v[220:223], v[176:179]\n"
    "v_mfma_f32_16x16x32_bf16 v[188:191], v[212:215], v[220:223], v[188:191]\n"
    "ds_read_b64_tr_b16 v[236:237], v247 offset:0\n"
    "ds_read_b64_tr_b16 v[238:239], v247 offset:8192\n"
    "v_mfma_f32_16x16x32_bf16 v[184:187], v[212:215], v[216:219], v[184:187]\n"
    "v_mfma_f32_16x16x32_bf16 v[180:183], v[212:215], v[232:235], v[180:183]\n"
    "s_waitcnt lgkmcnt(8)\n"
    "v_mfma_f32_16x16x32_bf16 v[0:3], v[204:207], v[192:195], v[0:3]\n"
    "ds_read_b64_tr_b16 v[240:241], v248 offset:0\n"
    "ds_read_b64_tr_b16 v[242:243], v248 offset:8192\n"
    "s_waitcnt lgkmcnt(9)\n"
    "v_mfma_f32_16x16x32_bf16 v[4:7], v[204:207], v[196:199], v[4:7]\n"
    "s_waitcnt lgkmcnt(8)\n"
    "v_mfma_f32_16x16x32_bf16 v[8:11], v[204:207], v[200:203], v[8:11]\n"
    "s_waitcnt lgkmcnt(6)\n"
    "v_mfma_f32_16x16x32_bf16 v[20:23], v[224:227], v[200:203], v[20:23]\n"
    "ds_read_b64_tr_b16 v[208:209], v249 offset:0\n"
    "ds_read_b64_tr_b16 v[210:211], v249 offset:8192\n"
    "v_mfma_f32_16x16x32_bf16 v[16:19], v[224:227], v[196:199], v[16:19]\n"
    "v_mfma_f32_16x16x32_bf16 v[12:15], v[224:227], v[192:195], v[12:15]\n"
    "s_waitcnt lgkmcnt(6)\n"
    "v_mfma_f32_16x16x32_bf16 v[24:27], v[228:231], v[192:195], v[24:27]\n"
    "ds_read_b64_tr_b16 v[212:213], v250 offset:0\n"
    "ds_read_b64_tr_b16 v[214:215], v250 offset:8192\n"
    "v_mfma_f32_16x16x32_bf16 v[28:31], v[228:231], v[196:199], v[28:31]\n"
    "ds_read_b128 v[232:235], v253 offset:0\n"
    "v_mfma_f32_16x16x32_bf16 v[32:35], v[228:231], v[200:203], v[32:35]\n"
    "s_waitcnt lgkmcnt(7)\n"
    "v_mfma_f32_16x16x32_bf16 v[44:47], v[236:239], v[200:203], v[44:47]\n"
    "ds_read_b64_tr_b16 v[216:217], v251 offset:0\n"
    "ds_read_b64_tr_b16 v[218:219], v251 offset:8192\n"
    "v_mfma_f32_16x16x32_bf16 v[40:43], v[236:239], v[196:199], v[40:43]\n"
    "ds_read_b128 v[220:223], v253 offset:2048\n"
    "v_mfma_f32_16x16x32_bf16 v[36:39], v[236:239], v[192:195], v[36:39]\n"
    "s_waitcnt lgkmcnt(8)\n"
    "v_mfma_f32_16x16x32_bf16 v[48:51], v[240:243], v[192:195], v[48:51]\n"
    "ds_read_b64_tr_b16 v[204:205], v244 offset:256\n"
    "ds_read_b64_tr_b16 v[206:207], v244 offset:8448\n"
    "v_mfma_f32_16x16x32_bf16 v[52:55], v[240:243], v[196:199], v[52:55]\n"
    "ds_read_b128 v[224:227], v253 offset:4096\n"
    "v_mfma_f32_16x16x32_bf16 v[56:59], v[240:243], v[200:203], v[56:59]\n"
    "s_waitcnt lgkmcnt(9)\n"
    "v_mfma_f32_16x16x32_bf16 v[68:71], v[208:211], v[200:203], v[68:71]\n"
    "ds_read_b64_tr_b16 v[228:229], v245 offset:256\n"
    "ds_read_b64_tr_b16 v[230:231], v245 offset:8448\n"
    "v_mfma_f32_16x16x32_bf16 v[64:67], v[208:211], v[196:199], v[64:67]\n"
    "v_mfma_f32_16x16x32_bf16 v[60:63], v[208:211], v[192:195], v[60:63]\n"
    "s_waitcnt lgkmcnt(9)\n"
    "v_mfma_f32_16x16x32_bf16 v[72:75], v[212:215], v[192:195], v[72:75]\n"
    "ds_read_b64_tr_b16 v[236:237], v246 offset:256\n"
    "ds_read_b64_tr_b16 v[238:239], v246 offset:8448\n"
    "v_mfma_f32_16x16x32_bf16 v[76:79], v[212:215], v[196:199], v[76:79]\n"
    "v_mfma_f32_16x16x32_bf16 v[80:83], v[212:215], v[200:203], v[80:83]\n"
    "s_waitcnt lgkmcnt(8)\n"
    "v_mfma_f32_16x16x32_bf16 v[92:95], v[216:219], v[200:203], v[92:95]\n"
    "ds_read_b64_tr_b16 v[240:241], v247 offset:256\n"
    "ds_read_b64_tr_b16 v[242:243], v247 offset:8448\n"
    "v_mfma_f32_16x16x32_bf16 v[88:91], v[216:219], v[196:199], v[88:91]\n"
    "v_mfma_f32_16x16x32_bf16 v[84:87], v[216:219], v[192:195], v[84:87]\n"
    "s_waitcnt lgkmcnt(7)\n"
    "v_mfma_f32_16x16x32_bf16 v[96:99], v[204:207], v[192:195], v[96:99]\n"
    "ds_read_b64_tr_b16 v[208:209], v248 offset:256\n"
    "ds_read_b64_tr_b16 v[210:211], v248 offset:8448\n"
    "v_mfma_f32_16x16x32_bf16 v[100:103], v[204:207], v[196:199], v[100:103]\n"
    "v_mfma_f32_16x16x32_bf16 v[104:107], v[204:207], v[200:203], v[104:107]\n"
    "s_waitcnt lgkmcnt(6)\n"
    "v_mfma_f32_16x16x32_bf16 v[116:119], v[228:231], v[200:203], v[116:119]\n"
    "ds_read_b64_tr_b16 v[212:213], v249 offset:256\n"
    "ds_read_b64_tr_b16 v[214:215], v249 offset:8448\n"
    "v_mfma_f32_16x16x32_bf16 v[112:115], v[228:231], v[196:199], v[112:115]\n"
    "v_mfma_f32_16x16x32_bf16 v[108:111], v[228:231], v[192:195], v[108:111]\n"
    "s_waitcnt lgkmcnt(6)\n"
    "v_mfma_f32_16x16x32_bf16 v[120:123], v[236:239], v[192:195], v[120:123]\n"
    "ds_read_b64_tr_b16 v[216:217], v250 offset:256\n"
    "ds_read_b64_tr_b16 v[218:219], v250 offset:8448\n"
    "v_mfma_f32_16x16x32_bf16 v[124:127], v[236:239], v[196:199], v[124:127]\n"
    "v_mfma_f32_16x16x32_bf16 v[128:131], v[236:239], v[200:203], v[128:131]\n"
    "s_waitcnt lgkmcnt(6)\n"
    "v_mfma_f32_16x16x32_bf16 v[140:143], v[240:243], v[200:203], v[140:143]\n"
    "ds_read_b64_tr_b16 v[204:205], v251 offset:256\n"
    "ds_read_b64_tr_b16 v[206:207], v251 offset:8448\n"
    "v_mfma_f32_16x16x32_bf16 v[136:139], v[240:243], v[196:199], v[136:139]\n"
    "v_mfma_f32_16x16x32_bf16 v[132:135], v[240:243], v[192:195], v[132:135]\n"
    "s_waitcnt lgkmcnt(6)\n"
    "v_mfma_f32_16x16x32_bf16 v[144:147], v[208:211], v[192:195], v[144:147]\n"
    "v_mfma_f32_16x16x32_bf16 v[148:151], v[208:211], v[196:199], v[148:151]\n"
    "v_mfma_f32_16x16x32_bf16 v[152:155], v[208:211], v[200:203], v[152:155]\n"
    "s_waitcnt lgkmcnt(0)\n"
    "s_barrier\n"
    "v_mfma_f32_16x16x32_bf16 v[164:167], v[212:215], v[200:203], v[164:167]\n"
    "ds_read_b64_tr_b16 v[228:229], v244 offset:16384\n"
    "ds_read_b64_tr_b16 v[230:231], v244 offset:24576\n"
    "v_mfma_f32_16x16x32_bf16 v[160:163], v[212:215], v[196:199], v[160:163]\n"
    "ds_read_b64_tr_b16 v[236:237], v245 offset:16384\n"
    "ds_read_b64_tr_b16 v[238:239], v245 offset:24576\n"
    "v_mfma_f32_16x16x32_bf16 v[156:159], v[212:215], v[192:195], v[156:159]\n"
    "v_mfma_f32_16x16x32_bf16 v[168:171], v[216:219], v[192:195], v[168:171]\n"
    "ds_read_b64_tr_b16 v[240:241], v246 offset:16384\n"
    "ds_read_b64_tr_b16 v[242:243], v246 offset:24576\n"
    "v_mfma_f32_16x16x32_bf16 v[172:175], v[216:219], v[196:199], v[172:175]\n"
    "v_mfma_f32_16x16x32_bf16 v[176:179], v[216:219], v[200:203], v[176:179]\n"
    "v_mfma_f32_16x16x32_bf16 v[188:191], v[204:207], v[200:203], v[188:191]\n"
    "ds_read_b64_tr_b16 v[208:209], v247 offset:16384\n"
    "ds_read_b64_tr_b16 v[210:211], v247 offset:24576\n"
    "v_mfma_f32_16x16x32_bf16 v[184:187], v[204:207], v[196:199], v[184:187]\n"
    "v_mfma_f32_16x16x32_bf16 v[180:183], v[204:207], v[192:195], v[180:183]\n"
    "s_waitcnt lgkmcnt(6)\n"
    "v_mfma_f32_16x16x32_bf16 v[0:3], v[228:231], v[232:235], v[0:3]\n"
    "ds_read_b64_tr_b16 v[212:213], v248 offset:16384\n"
    "ds_read_b64_tr_b16 v[214:215], v248 offset:24576\n"
    "v_mfma_f32_16x16x32_bf16 v[4:7], v[228:231], v[220:223], v[4:7]\n"
    "v_mfma_f32_16x16x32_bf16 v[8:11], v[228:231], v[224:227], v[8:11]\n"
    "s_waitcnt lgkmcnt(6)\n"
    "v_mfma_f32_16x16x32_bf16 v[20:23], v[236:239], v[224:227], v[20:23]\n"
    "ds_read_b64_tr_b16 v[216:217], v249 offset:16384\n"
    "ds_read_b64_tr_b16 v[218:219], v249 offset:24576\n"
    "v_mfma_f32_16x16x32_bf16 v[16:19], v[236:239], v[220:223], v[16:19]\n"
    "v_mfma_f32_16x16x32_bf16 v[12:15], v[236:239], v[232:235], v[12:15]\n"
    "s_waitcnt lgkmcnt(6)\n"
    "v_mfma_f32_16x16x32_bf16 v[24:27], v[240:243], v[232:235], v[24:27]\n"
    "ds_read_b64_tr_b16 v[204:205], v250 offset:16384\n"
    "ds_read_b64_tr_b16 v[206:207], v250 offset:24576\n"
    "v_mfma_f32_16x16x32_bf16 v[28:31], v[240:243], v[220:223], v[28:31]\n"
    "v_mfma_f32_16x16x32_bf16 v[32:35], v[240:243], v[224:227], v[32:35]\n"
    "s_waitcnt lgkmcnt(6)\n"
    "v_mfma_f32_16x16x32_bf16 v[44:47], v[208:211], v[224:227], v[44:47]\n"
    "ds_read_b64_tr_b16 v[192:193], v251 offset:16384\n"
    "ds_read_b64_tr_b16 v[194:195], v251 offset:24576\n"
    "v_mfma_f32_16x16x32_bf16 v[40:43], v[208:211], v[220:223], v[40:43]\n"
    "v_mfma_f32_16x16x32_bf16 v[36:39], v[208:211], v[232:235], v[36:39]\n"
    "s_waitcnt lgkmcnt(6)\n"
    "v_mfma_f32_16x16x32_bf16 v[48:51], v[212:215], v[232:235], v[48:51]\n"
    "ds_read_b64_tr_b16 v[196:197], v244 offset:16640\n"
    "ds_read_b64_tr_b16 v[198:199], v244 offset:24832\n"
    "v_mfma_f32_16x16x32_bf16 v[52:55], v[212:215], v[220:223], v[52:55]\n"
    "v_mfma_f32_16x16x32_bf16 v[56:59], v[212:215], v[224:227], v[56:59]\n"
    "s_waitcnt lgkmcnt(6)\n"
    "v_mfma_f32_16x16x32_bf16 v[68:71], v[216:219], v[224:227], v[68:71]\n"
    "ds_read_b64_tr_b16 v[200:201], v245 offset:16640\n"
    "ds_read_b64_tr_b16 v[202:203], v245 offset:24832\n"
    "v_mfma_f32_16x16x32_bf16 v[64:67], v[216:219], v[220:223], v[64:67]\n"
    "v_mfma_f32_16x16x32_bf16 v[60:63], v[216:219], v[232:235], v[60:63]\n"
    "s_waitcnt lgkmcnt(6)\n"
    "v_mfma_f32_16x16x32_bf16 v[72:75], v[204:207], v[232:235], v[72:75]\n"
    "ds_read_b64_tr_b16 v[228:229], v246 offset:16640\n"
    "ds_read_b64_tr_b16 v[230:231], v246 offset:24832\n"
    "v_mfma_f32_16x16x32_bf16 v[76:79], v[204:207], v[220:223], v[76:79]\n"
    "v_mfma_f32_16x16x32_bf16 v[80:83], v[204:207], v[224:227], v[80:83]\n"
    "s_waitcnt lgkmcnt(6)\n"
    "v_mfma_f32_16x16x32_bf16 v[92:95], v[192:195], v[224:227], v[92:95]\n"
    "ds_read_b64_tr_b16 v[236:237], v247 offset:16640\n"
    "ds_read_b64_tr_b16 v[238:239], v247 offset:24832\n"
    "v_mfma_f32_16x16x32_bf16 v[88:91], v[192:195], v[220:223], v[88:91]\n"
    "v_mfma_f32_16x16x32_bf16 v[84:87], v[192:195], v[232:235], v[84:87]\n"
    "s_waitcnt lgkmcnt(6)\n"
    "v_mfma_f32_16x16x32_bf16 v[96:99], v[196:199], v[232:235], v[96:99]\n"
    "ds_read_b64_tr_b16 v[240:241], v248 offset:16640\n"
    "ds_read_b64_tr_b16 v[242:243], v248 offset:24832\n"
    "v_mfma_f32_16x16x32_bf16 v[100:103], v[196:199], v[220:223], v[100:103]\n"
    "v_mfma_f32_16x16x32_bf16 v[104:107], v[196:199], v[224:227], v[104:107]\n"
    "s_waitcnt lgkmcnt(6)\n"
    "v_mfma_f32_16x16x32_bf16 v[116:119], v[200:203], v[224:227], v[116:119]\n"
    "ds_read_b64_tr_b16 v[208:209], v249 offset:16640\n"
    "ds_read_b64_tr_b16 v[210:211], v249 offset:24832\n"
    "v_mfma_f32_16x16x32_bf16 v[112:115], v[200:203], v[220:223], v[112:115]\n"
    "v_mfma_f32_16x16x32_bf16 v[108:111], v[200:203], v[232:235], v[108:111]\n"
    "s_waitcnt lgkmcnt(6)\n"
    "v_mfma_f32_16x16x32_bf16 v[120:123], v[228:231], v[232:235], v[120:123]\n"
    "ds_read_b64_tr_b16 v[212:213], v250 offset:16640\n"
    "ds_read_b64_tr_b16 v[214:215], v250 offset:24832\n"
    "v_mfma_f32_16x16x32_bf16 v[124:127], v[228:231], v[220:223], v[124:127]\n"
    "v_mfma_f32_16x16x32_bf16 v[128:131], v[228:231], v[224:227], v[128:131]\n"
    "s_waitcnt lgkmcnt(6)\n"
    "v_mfma_f32_16x16x32_bf16 v[140:143], v[236:239], v[224:227], v[140:143]\n"
    "ds_read_b64_tr_b16 v[216:217], v251 offset:16640\n"
    "ds_read_b64_tr_b16 v[218:219], v251 offset:24832\n"
    "v_mfma_f32_16x16x32_bf16 v[136:139], v[236:239], v[220:223], v[136:139]\n"
    "v_mfma_f32_16x16x32_bf16 v[132:135], v[236:239], v[232:235], v[132:135]\n"
    "s_waitcnt lgkmcnt(6)\n"
    "v_mfma_f32_16x16x32_bf16 v[144:147], v[240:243], v[232:235], v[144:147]\n"
    "v_mfma_f32_16x16x32_bf16 v[148:151], v[240:243], v[220:223], v[148:151]\n"
    "v_mfma_f32_16x16x32_bf16 v[152:155], v[240:243], v[224:227], v[152:155]\n"
    "s_waitcnt lgkmcnt(0)\n"
    "s_barrier\n"
    "v_mfma_f32_16x16x32_bf16 v[164:167], v[208:211], v[224:227], v[164:167]\n"
    "ds_read_b64_tr_b16 v[204:205], v244 offset:32768\n"
    "ds_read_b64_tr_b16 v[206:207], v244 offset:40960\n"
    "v_mfma_f32_16x16x32_bf16 v[160:163], v[208:211], v[220:223], v[160:163]\n"
    "ds_read_b128 v[192:195], v252 offset:6144\n"
    "ds_read_b128 v[196:199], v252 offset:8192\n"
    "v_mfma_f32_16x16x32_bf16 v[156:159], v[208:211], v[232:235], v[156:159]\n"
    "ds_read_b128 v[200:203], v252 offset:10240\n"
    "ds_read_b64_tr_b16 v[228:229], v245 offset:32768\n"
    "v_mfma_f32_16x16x32_bf16 v[168:171], v[212:215], v[232:235], v[168:171]\n"
    "ds_read_b64_tr_b16 v[230:231], v245 offset:40960\n"
    "ds_read_b64_tr_b16 v[236:237], v246 offset:32768\n"
    "v_mfma_f32_16x16x32_bf16 v[172:175], v[212:215], v[220:223], v[172:175]\n"
    "ds_read_b64_tr_b16 v[238:239], v246 offset:40960\n"
    "v_mfma_f32_16x16x32_bf16 v[176:179], v[212:215], v[224:227], v[176:179]\n"
    "v_mfma_f32_16x16x32_bf16 v[188:191], v[216:219], v[224:227], v[188:191]\n"
    "ds_read_b64_tr_b16 v[240:241], v247 offset:32768\n"
    "ds_read_b64_tr_b16 v[242:243], v247 offset:40960\n"
    "v_mfma_f32_16x16x32_bf16 v[184:187], v[216:219], v[220:223], v[184:187]\n"
    "v_mfma_f32_16x16x32_bf16 v[180:183], v[216:219], v[232:235], v[180:183]\n"
    "s_waitcnt lgkmcnt(8)\n"
    "v_mfma_f32_16x16x32_bf16 v[0:3], v[204:207], v[192:195], v[0:3]\n"
    "ds_read_b64_tr_b16 v[208:209], v248 offset:32768\n"
    "ds_read_b64_tr_b16 v[210:211], v248 offset:40960\n"
    "s_waitcnt lgkmcnt(9)\n"
    "v_mfma_f32_16x16x32_bf16 v[4:7], v[204:207], v[196:199], v[4:7]\n"
    "s_waitcnt lgkmcnt(8)\n"
    "v_mfma_f32_16x16x32_bf16 v[8:11], v[204:207], v[200:203], v[8:11]\n"
    "s_waitcnt lgkmcnt(6)\n"
    "v_mfma_f32_16x16x32_bf16 v[20:23], v[228:231], v[200:203], v[20:23]\n"
    "ds_read_b64_tr_b16 v[212:213], v249 offset:32768\n"
    "ds_read_b64_tr_b16 v[214:215], v249 offset:40960\n"
    "v_mfma_f32_16x16x32_bf16 v[16:19], v[228:231], v[196:199], v[16:19]\n"
    "v_mfma_f32_16x16x32_bf16 v[12:15], v[228:231], v[192:195], v[12:15]\n"
    "s_waitcnt lgkmcnt(6)\n"
    "v_mfma_f32_16x16x32_bf16 v[24:27], v[236:239], v[192:195], v[24:27]\n"
    "ds_read_b64_tr_b16 v[216:217], v250 offset:32768\n"
    "ds_read_b64_tr_b16 v[218:219], v250 offset:40960\n"
    "v_mfma_f32_16x16x32_bf16 v[28:31], v[236:239], v[196:199], v[28:31]\n"
    "ds_read_b128 v[232:235], v253 offset:6144\n"
    "v_mfma_f32_16x16x32_bf16 v[32:35], v[236:239], v[200:203], v[32:35]\n"
    "s_waitcnt lgkmcnt(7)\n"
    "v_mfma_f32_16x16x32_bf16 v[44:47], v[240:243], v[200:203], v[44:47]\n"
    "ds_read_b64_tr_b16 v[220:221], v251 offset:32768\n"
    "ds_read_b64_tr_b16 v[222:223], v251 offset:40960\n"
    "v_mfma_f32_16x16x32_bf16 v[40:43], v[240:243], v[196:199], v[40:43]\n"
    "ds_read_b128 v[224:227], v253 offset:8192\n"
    "v_mfma_f32_16x16x32_bf16 v[36:39], v[240:243], v[192:195], v[36:39]\n"
    "s_waitcnt lgkmcnt(8)\n"
    "v_mfma_f32_16x16x32_bf16 v[48:51], v[208:211], v[192:195], v[48:51]\n"
    "ds_read_b64_tr_b16 v[204:205], v244 offset:33024\n"
    "ds_read_b64_tr_b16 v[206:207], v244 offset:41216\n"
    "v_mfma_f32_16x16x32_bf16 v[52:55], v[208:211], v[196:199], v[52:55]\n"
    "ds_read_b128 v[228:231], v253 offset:10240\n"
    "v_mfma_f32_16x16x32_bf16 v[56:59], v[208:211], v[200:203], v[56:59]\n"
    "s_waitcnt lgkmcnt(9)\n"
    "v_mfma_f32_16x16x32_bf16 v[68:71], v[212:215], v[200:203], v[68:71]\n"
    "ds_read_b64_tr_b16 v[236:237], v245 offset:33024\n"
    "ds_read_b64_tr_b16 v[238:239], v245 offset:41216\n"
    "v_mfma_f32_16x16x32_bf16 v[64:67], v[212:215], v[196:199], v[64:67]\n"
    "v_mfma_f32_16x16x32_bf16 v[60:63], v[212:215], v[192:195], v[60:63]\n"
    "s_waitcnt lgkmcnt(9)\n"
    "v_mfma_f32_16x16x32_bf16 v[72:75], v[216:219], v[192:195], v[72:75]\n"
    "ds_read_b64_tr_b16 v[240:241], v246 offset:33024\n"
    "ds_read_b64_tr_b16 v[242:243], v246 offset:41216\n"
    "v_mfma_f32_16x16x32_bf16 v[76:79], v[216:219], v[196:199], v[76:79]\n"
    "v_mfma_f32_16x16x32_bf16 v[80:83], v[216:219], v[200:203], v[80:83]\n"
    "s_waitcnt lgkmcnt(8)\n"
    "v_mfma_f32_16x16x32_bf16 v[92:95], v[220:223], v[200:203], v[92:95]\n"
    "ds_read_b64_tr_b16 v[208:209], v247 offset:33024\n"
    "ds_read_b64_tr_b16 v[210:211], v247 offset:41216\n"
    "v_mfma_f32_16x16x32_bf16 v[88:91], v[220:223], v[196:199], v[88:91]\n"
    "v_mfma_f32_16x16x32_bf16 v[84:87], v[220:223], v[192:195], v[84:87]\n"
    "s_waitcnt lgkmcnt(7)\n"
    "v_mfma_f32_16x16x32_bf16 v[96:99], v[204:207], v[192:195], v[96:99]\n"
    "ds_read_b64_tr_b16 v[212:213], v248 offset:33024\n"
    "ds_read_b64_tr_b16 v[214:215], v248 offset:41216\n"
    "v_mfma_f32_16x16x32_bf16 v[100:103], v[204:207], v[196:199], v[100:103]\n"
    "v_mfma_f32_16x16x32_bf16 v[104:107], v[204:207], v[200:203], v[104:107]\n"
    "s_waitcnt lgkmcnt(6)\n"
    "v_mfma_f32_16x16x32_bf16 v[116:119], v[236:239], v[200:203], v[116:119]\n"
    "ds_read_b64_tr_b16 v[216:217], v249 offset:33024\n"
    "ds_read_b64_tr_b16 v[218:219], v249 offset:41216\n"
    "v_mfma_f32_16x16x32_bf16 v[112:115], v[236:239], v[196:199], v[112:115]\n"
    "v_mfma_f32_16x16x32_bf16 v[108:111], v[236:239], v[192:195], v[108:111]\n"
    "s_waitcnt lgkmcnt(6)\n"
    "v_mfma_f32_16x16x32_bf16 v[120:123], v[240:243], v[192:195], v[120:123]\n"
    "ds_read_b64_tr_b16 v[220:221], v250 offset:33024\n"
    "ds_read_b64_tr_b16 v[222:223], v250 offset:41216\n"
    "v_mfma_f32_16x16x32_bf16 v[124:127], v[240:243], v[196:199], v[124:127]\n"
    "v_mfma_f32_16x16x32_bf16 v[128:131], v[240:243], v[200:203], v[128:131]\n"
    "s_waitcnt lgkmcnt(6)\n"
    "v_mfma_f32_16x16x32_bf16 v[140:143], v[208:211], v[200:203], v[140:143]\n"
    "ds_read_b64_tr_b16 v[204:205], v251 offset:33024\n"
    "ds_read_b64_tr_b16 v[206:207], v251 offset:41216\n"
    "v_mfma_f32_16x16x32_bf16 v[136:139], v[208:211], v[196:199], v[136:139]\n"
    "v_mfma_f32_16x16x32_bf16 v[132:135], v[208:211], v[192:195], v[132:135]\n"
    "s_waitcnt lgkmcnt(6)\n"
    "v_mfma_f32_16x16x32_bf16 v[144:147], v[212:215], v[192:195], v[144:147]\n"
    "v_mfma_f32_16x16x32_bf16 v[148:151], v[212:215], v[196:199], v[148:151]\n"
    "v_mfma_f32_16x16x32_bf16 v[152:155], v[212:215], v[200:203], v[152:155]\n"
    "s_waitcnt lgkmcnt(0)\n"
    "s_barrier\n"
    "v_mfma_f32_16x16x32_bf16 v[164:167], v[216:219], v[200:203], v[164:167]\n"
    "ds_read_b64_tr_b16 v[236:237], v244 offset:49152\n"
    "ds_read_b64_tr_b16 v[238:239], v244 offset:57344\n"
    "v_mfma_f32_16x16x32_bf16 v[160:163], v[216:219], v[196:199], v[160:163]\n"
    "ds_read_b64_tr_b16 v[240:241], v245 offset:49152\n"
    "ds_read_b64_tr_b16 v[242:243], v245 offset:57344\n"
    "v_mfma_f32_16x16x32_bf16 v[156:159], v[216:219], v[192:195], v[156:159]\n"
    "v_mfma_f32_16x16x32_bf16 v[168:171], v[220:223], v[192:195], v[168:171]\n"
    "ds_read_b64_tr_b16 v[208:209], v246 offset:49152\n"
    "ds_read_b64_tr_b16 v[210:211], v246 offset:57344\n"
    "v_mfma_f32_16x16x32_bf16 v[172:175], v[220:223], v[196:199], v[172:175]\n"
    "v_mfma_f32_16x16x32_bf16 v[176:179], v[220:223], v[200:203], v[176:179]\n"
    "v_mfma_f32_16x16x32_bf16 v[188:191], v[204:207], v[200:203], v[188:191]\n"
    "ds_read_b64_tr_b16 v[212:213], v247 offset:49152\n"
    "ds_read_b64_tr_b16 v[214:215], v247 offset:57344\n"
    "v_mfma_f32_16x16x32_bf16 v[184:187], v[204:207], v[196:199], v[184:187]\n"
    "v_mfma_f32_16x16x32_bf16 v[180:183], v[204:207], v[192:195], v[180:183]\n"
    "s_waitcnt lgkmcnt(6)\n"
    "v_mfma_f32_16x16x32_bf16 v[0:3], v[236:239], v[232:235], v[0:3]\n"
    "ds_read_b64_tr_b16 v[216:217], v248 offset:49152\n"
    "ds_read_b64_tr_b16 v[218:219], v248 offset:57344\n"
    "v_mfma_f32_16x16x32_bf16 v[4:7], v[236:239], v[224:227], v[4:7]\n"
    "v_mfma_f32_16x16x32_bf16 v[8:11], v[236:239], v[228:231], v[8:11]\n"
    "s_waitcnt lgkmcnt(6)\n"
    "v_mfma_f32_16x16x32_bf16 v[20:23], v[240:243], v[228:231], v[20:23]\n"
    "ds_read_b64_tr_b16 v[220:221], v249 offset:49152\n"
    "ds_read_b64_tr_b16 v[222:223], v249 offset:57344\n"
    "v_mfma_f32_16x16x32_bf16 v[16:19], v[240:243], v[224:227], v[16:19]\n"
    "v_mfma_f32_16x16x32_bf16 v[12:15], v[240:243], v[232:235], v[12:15]\n"
    "s_waitcnt lgkmcnt(6)\n"
    "v_mfma_f32_16x16x32_bf16 v[24:27], v[208:211], v[232:235], v[24:27]\n"
    "ds_read_b64_tr_b16 v[204:205], v250 offset:49152\n"
    "ds_read_b64_tr_b16 v[206:207], v250 offset:57344\n"
    "v_mfma_f32_16x16x32_bf16 v[28:31], v[208:211], v[224:227], v[28:31]\n"
    "v_mfma_f32_16x16x32_bf16 v[32:35], v[208:211], v[228:231], v[32:35]\n"
    "s_waitcnt lgkmcnt(6)\n"
    "v_mfma_f32_16x16x32_bf16 v[44:47], v[212:215], v[228:231], v[44:47]\n"
    "ds_read_b64_tr_b16 v[192:193], v251 offset:49152\n"
    "ds_read_b64_tr_b16 v[194:195], v251 offset:57344\n"
    "v_mfma_f32_16x16x32_bf16 v[40:43], v[212:215], v[224:227], v[40:43]\n"
    "v_mfma_f32_16x16x32_bf16 v[36:39], v[212:215], v[232:235], v[36:39]\n"
    "s_waitcnt lgkmcnt(6)\n"
    "v_mfma_f32_16x16x32_bf16 v[48:51], v[216:219], v[232:235], v[48:51]\n"
    "ds_read_b64_tr_b16 v[196:197], v244 offset:49408\n"
    "ds_read_b64_tr_b16 v[198:199], v244 offset:57600\n"
    "v_mfma_f32_16x16x32_bf16 v[52:55], v[216:219], v[224:227], v[52:55]\n"
    "v_mfma_f32_16x16x32_bf16 v[56:59], v[216:219], v[228:231], v[56:59]\n"
    "s_waitcnt lgkmcnt(6)\n"
    "v_mfma_f32_16x16x32_bf16 v[68:71], v[220:223], v[228:231], v[68:71]\n"
    "ds_read_b64_tr_b16 v[200:201], v245 offset:49408\n"
    "ds_read_b64_tr_b16 v[202:203], v245 offset:57600\n"
    "v_mfma_f32_16x16x32_bf16 v[64:67], v[220:223], v[224:227], v[64:67]\n"
    "v_mfma_f32_16x16x32_bf16 v[60:63], v[220:223], v[232:235], v[60:63]\n"
    "s_waitcnt lgkmcnt(6)\n"
    "v_mfma_f32_16x16x32_bf16 v[72:75], v[204:207], v[232:235], v[72:75]\n"
    "ds_read_b64_tr_b16 v[236:237], v246 offset:49408\n"
    "ds_read_b64_tr_b16 v[238:239], v246 offset:57600\n"
    "v_mfma_f32_16x16x32_bf16 v[76:79], v[204:207], v[224:227], v[76:79]\n"
    "v_mfma_f32_16x16x32_bf16 v[80:83], v[204:207], v[228:231], v[80:83]\n"
    "s_waitcnt lgkmcnt(6)\n"
    "v_mfma_f32_16x16x32_bf16 v[92:95], v[192:195], v[228:231], v[92:95]\n"
    "ds_read_b64_tr_b16 v[240:241], v247 offset:49408\n"
    "ds_read_b64_tr_b16 v[242:243], v247 offset:57600\n"
    "v_mfma_f32_16x16x32_bf16 v[88:91], v[192:195], v[224:227], v[88:91]\n"
    "v_mfma_f32_16x16x32_bf16 v[84:87], v[192:195], v[232:235], v[84:87]\n"
    "s_waitcnt lgkmcnt(6)\n"
    "v_mfma_f32_16x16x32_bf16 v[96:99], v[196:199], v[232:235], v[96:99]\n"
    "ds_read_b64_tr_b16 v[208:209], v248 offset:49408\n"
    "ds_read_b64_tr_b16 v[210:211], v248 offset:57600\n"
    "v_mfma_f32_16x16x32_bf16 v[100:103], v[196:199], v[224:227], v[100:103]\n"
    "v_mfma_f32_16x16x32_bf16 v[104:107], v[196:199], v[228:231], v[104:107]\n"
    "s_waitcnt lgkmcnt(6)\n"
    "v_mfma_f32_16x16x32_bf16 v[116:119], v[200:203], v[228:231], v[116:119]\n"
    "ds_read_b64_tr_b16 v[212:213], v249 offset:49408\n"
    "ds_read_b64_tr_b16 v[214:215], v249 offset:57600\n"
    "v_mfma_f32_16x16x32_bf16 v[112:115], v[200:203], v[224:227], v[112:115]\n"
    "v_mfma_f32_16x16x32_bf16 v[108:111], v[200:203], v[232:235], v[108:111]\n"
    "s_waitcnt lgkmcnt(6)\n"
    "v_mfma_f32_16x16x32_bf16 v[120:123], v[236:239], v[232:235], v[120:123]\n"
    "ds_read_b64_tr_b16 v[216:217], v250 offset:49408\n"
    "ds_read_b64_tr_b16 v[218:219], v250 offset:57600\n"
    "v_mfma_f32_16x16x32_bf16 v[124:127], v[236:239], v[224:227], v[124:127]\n"
    "v_mfma_f32_16x16x32_bf16 v[128:131], v[236:239], v[228:231], v[128:131]\n"
    "s_waitcnt lgkmcnt(6)\n"
    "v_mfma_f32_16x16x32_bf16 v[140:143], v[240:243], v[228:231], v[140:143]\n"
    "ds_read_b64_tr_b16 v[220:221], v251 offset:49408\n"
    "ds_read_b64_tr_b16 v[222:223], v251 offset:57600\n"
    "v_mfma_f32_16x16x32_bf16 v[136:139], v[240:243], v[224:227], v[136:139]\n"
    "v_mfma_f32_16x16x32_bf16 v[132:135], v[240:243], v[232:235], v[132:135]\n"
    "s_waitcnt lgkmcnt(6)\n"
    "v_mfma_f32_16x16x32_bf16 v[144:147], v[208:211], v[232:235], v[144:147]\n"
    "v_mfma_f32_16x16x32_bf16 v[148:151], v[208:211], v[224:227], v[148:151]\n"
    "v_mfma_f32_16x16x32_bf16 v[152:155], v[208:211], v[228:231], v[152:155]\n"
    "s_waitcnt lgkmcnt(0)\n"
    "s_barrier\n"
    "v_mfma_f32_16x16x32_bf16 v[164:167], v[212:215], v[228:231], v[164:167]\n"
    "ds_read_b64_tr_b16 v[204:205], v244 offset:0\n"
    "ds_read_b64_tr_b16 v[206:207], v244 offset:8192\n"
    "v_mfma_f32_16x16x32_bf16 v[160:163], v[212:215], v[224:227], v[160:163]\n"
    "ds_read_b128 v[192:195], v252 offset:0\n"
    "ds_read_b128 v[196:199], v252 offset:2048\n"
    "v_mfma_f32_16x16x32_bf16 v[156:159], v[212:215], v[232:235], v[156:159]\n"
    "ds_read_b128 v[200:203], v252 offset:4096\n"
    "ds_read_b64_tr_b16 v[236:237], v245 offset:0\n"
    "v_mfma_f32_16x16x32_bf16 v[168:171], v[216:219], v[232:235], v[168:171]\n"
    "ds_read_b64_tr_b16 v[238:239], v245 offset:8192\n"
    "ds_read_b64_tr_b16 v[240:241], v246 offset:0\n"
    "v_mfma_f32_16x16x32_bf16 v[172:175], v[216:219], v[224:227], v[172:175]\n"
    "ds_read_b64_tr_b16 v[242:243], v246 offset:8192\n"
    "v_mfma_f32_16x16x32_bf16 v[176:179], v[216:219], v[228:231], v[176:179]\n"
    "v_mfma_f32_16x16x32_bf16 v[188:191], v[220:223], v[228:231], v[188:191]\n"
    "ds_read_b64_tr_b16 v[208:209], v247 offset:0\n"
    "ds_read_b64_tr_b16 v[210:211], v247 offset:8192\n"
    "v_mfma_f32_16x16x32_bf16 v[184:187], v[220:223], v[224:227], v[184:187]\n"
    "v_mfma_f32_16x16x32_bf16 v[180:183], v[220:223], v[232:235], v[180:183]\n"
    "s_waitcnt lgkmcnt(8)\n"
    "v_mfma_f32_16x16x32_bf16 v[0:3], v[204:207], v[192:195], v[0:3]\n"
    "ds_read_b64_tr_b16 v[212:213], v248 offset:0\n"
    "ds_read_b64_tr_b16 v[214:215], v248 offset:8192\n"
    "s_waitcnt lgkmcnt(9)\n"
    "v_mfma_f32_16x16x32_bf16 v[4:7], v[204:207], v[196:199], v[4:7]\n"
    "s_waitcnt lgkmcnt(8)\n"
    "v_mfma_f32_16x16x32_bf16 v[8:11], v[204:207], v[200:203], v[8:11]\n"
    "s_waitcnt lgkmcnt(6)\n"
    "v_mfma_f32_16x16x32_bf16 v[20:23], v[236:239], v[200:203], v[20:23]\n"
    "ds_read_b64_tr_b16 v[216:217], v249 offset:0\n"
    "ds_read_b64_tr_b16 v[218:219], v249 offset:8192\n"
    "v_mfma_f32_16x16x32_bf16 v[16:19], v[236:239], v[196:199], v[16:19]\n"
    "v_mfma_f32_16x16x32_bf16 v[12:15], v[236:239], v[192:195], v[12:15]\n"
    "s_waitcnt lgkmcnt(6)\n"
    "v_mfma_f32_16x16x32_bf16 v[24:27], v[240:243], v[192:195], v[24:27]\n"
    "ds_read_b64_tr_b16 v[220:221], v250 offset:0\n"
    "ds_read_b64_tr_b16 v[222:223], v250 offset:8192\n"
    "v_mfma_f32_16x16x32_bf16 v[28:31], v[240:243], v[196:199], v[28:31]\n"
    "ds_read_b128 v[232:235], v253 offset:0\n"
    "v_mfma_f32_16x16x32_bf16 v[32:35], v[240:243], v[200:203], v[32:35]\n"
    "s_waitcnt lgkmcnt(7)\n"
    "v_mfma_f32_16x16x32_bf16 v[44:47], v[208:211], v[200:203], v[44:47]\n"
    "ds_read_b64_tr_b16 v[224:225], v251 offset:0\n"
    "ds_read_b64_tr_b16 v[226:227], v251 offset:8192\n"
    "v_mfma_f32_16x16x32_bf16 v[40:43], v[208:211], v[196:199], v[40:43]\n"
    "ds_read_b128 v[228:231], v253 offset:2048\n"
    "v_mfma_f32_16x16x32_bf16 v[36:39], v[208:211], v[192:195], v[36:39]\n"
    "s_waitcnt lgkmcnt(8)\n"
    "v_mfma_f32_16x16x32_bf16 v[48:51], v[212:215], v[192:195], v[48:51]\n"
    "ds_read_b64_tr_b16 v[204:205], v244 offset:256\n"
    "ds_read_b64_tr_b16 v[206:207], v244 offset:8448\n"
    "v_mfma_f32_16x16x32_bf16 v[52:55], v[212:215], v[196:199], v[52:55]\n"
    "ds_read_b128 v[236:239], v253 offset:4096\n"
    "v_mfma_f32_16x16x32_bf16 v[56:59], v[212:215], v[200:203], v[56:59]\n"
    "s_waitcnt lgkmcnt(9)\n"
    "v_mfma_f32_16x16x32_bf16 v[68:71], v[216:219], v[200:203], v[68:71]\n"
    "ds_read_b64_tr_b16 v[240:241], v245 offset:256\n"
    "ds_read_b64_tr_b16 v[242:243], v245 offset:8448\n"
    "v_mfma_f32_16x16x32_bf16 v[64:67], v[216:219], v[196:199], v[64:67]\n"
    "v_mfma_f32_16x16x32_bf16 v[60:63], v[216:219], v[192:195], v[60:63]\n"
    "s_waitcnt lgkmcnt(9)\n"
    "v_mfma_f32_16x16x32_bf16 v[72:75], v[220:223], v[192:195], v[72:75]\n"
    "ds_read_b64_tr_b16 v[208:209], v246 offset:256\n"
    "ds_read_b64_tr_b16 v[210:211], v246 offset:8448\n"
    "v_mfma_f32_16x16x32_bf16 v[76:79], v[220:223], v[196:199], v[76:79]\n"
    "v_mfma_f32_16x16x32_bf16 v[80:83], v[220:223], v[200:203], v[80:83]\n"
    "s_waitcnt lgkmcnt(8)\n"
    "v_mfma_f32_16x16x32_bf16 v[92:95], v[224:227], v[200:203], v[92:95]\n"
    "ds_read_b64_tr_b16 v[212:213], v247 offset:256\n"
    "ds_read_b64_tr_b16 v[214:215], v247 offset:8448\n"
    "v_mfma_f32_16x16x32_bf16 v[88:91], v[224:227], v[196:199], v[88:91]\n"
    "v_mfma_f32_16x16x32_bf16 v[84:87], v[224:227], v[192:195], v[84:87]\n"
    "s_waitcnt lgkmcnt(7)\n"
    "v_mfma_f32_16x16x32_bf16 v[96:99], v[204:207], v[192:195], v[96:99]\n"
    "ds_read_b64_tr_b16 v[216:217], v248 offset:256\n"
    "ds_read_b64_tr_b16 v[218:219], v248 offset:8448\n"
    "v_mfma_f32_16x16x32_bf16 v[100:103], v[204:207], v[196:199], v[100:103]\n"
    "v_mfma_f32_16x16x32_bf16 v[104:107], v[204:207], v[200:203], v[104:107]\n"
    "s_waitcnt lgkmcnt(6)\n"
    "v_mfma_f32_16x16x32_bf16 v[116:119], v[240:243], v[200:203], v[116:119]\n"
    "ds_read_b64_tr_b16 v[220:221], v249 offset:256\n"
    "ds_read_b64_tr_b16 v[222:223], v249 offset:8448\n"
    "v_mfma_f32_16x16x32_bf16 v[112:115], v[240:243], v[196:199], v[112:115]\n"
    "v_mfma_f32_16x16x32_bf16 v[108:111], v[240:243], v[192:195], v[108:111]\n"
    "s_waitcnt lgkmcnt(6)\n"
    "v_mfma_f32_16x16x32_bf16 v[120:123], v[208:211], v[192:195], v[120:123]\n"
    "ds_read_b64_tr_b16 v[224:225], v250 offset:256\n"
    "ds_read_b64_tr_b16 v[226:227], v250 offset:8448\n"
    "v_mfma_f32_16x16x32_bf16 v[124:127], v[208:211], v[196:199], v[124:127]\n"
    "v_mfma_f32_16x16x32_bf16 v[128:131], v[208:211], v[200:203], v[128:131]\n"
    "s_waitcnt lgkmcnt(6)\n"
    "v_mfma_f32_16x16x32_bf16 v[140:143], v[212:215], v[200:203], v[140:143]\n"
    "ds_read_b64_tr_b16 v[204:205], v251 offset:256\n"
    "ds_read_b64_tr_b16 v[206:207], v251 offset:8448\n"
    "v_mfma_f32_16x16x32_bf16 v[136:139], v[212:215], v[196:199], v[136:139]\n"
    "v_mfma_f32_16x16x32_bf16 v[132:135], v[212:215], v[192:195], v[132:135]\n"
    "s_waitcnt lgkmcnt(6)\n"
    "v_mfma_f32_16x16x32_bf16 v[144:147], v[216:219], v[192:195], v[144:147]\n"
    "v_mfma_f32_16x16x32_bf16 v[148:151], v[216:219], v[196:199], v[148:151]\n"
    "v_mfma_f32_16x16x32_bf16 v[152:155], v[216:219], v[200:203], v[152:155]\n"
    "s_waitcnt lgkmcnt(0)\n"
    "s_barrier\n"
    "v_xor_b32 v241, 32, v254\n"
    "v_xor_b32 v242, 64, v254\n"
    "v_xor_b32 v243, 96, v254\n"
    "v_mfma_f32_16x16x32_bf16 v[164:167], v[220:223], v[200:203], v[164:167]\n"
    "ds_read_b64_tr_b16 v[208:209], v244 offset:16384\n"
    "ds_read_b64_tr_b16 v[210:211], v244 offset:24576\n"
    "v_mfma_f32_16x16x32_bf16 v[160:163], v[220:223], v[196:199], v[160:163]\n"
    "ds_read_b64_tr_b16 v[212:213], v245 offset:16384\n"
    "ds_read_b64_tr_b16 v[214:215], v245 offset:24576\n"
    "v_mfma_f32_16x16x32_bf16 v[156:159], v[220:223], v[192:195], v[156:159]\n"
    "v_mfma_f32_16x16x32_bf16 v[168:171], v[224:227], v[192:195], v[168:171]\n"
    "ds_read_b64_tr_b16 v[216:217], v246 offset:16384\n"
    "ds_read_b64_tr_b16 v[218:219], v246 offset:24576\n"
    "v_mfma_f32_16x16x32_bf16 v[172:175], v[224:227], v[196:199], v[172:175]\n"
    "v_mfma_f32_16x16x32_bf16 v[176:179], v[224:227], v[200:203], v[176:179]\n"
    "v_mfma_f32_16x16x32_bf16 v[188:191], v[204:207], v[200:203], v[188:191]\n"
    "ds_read_b64_tr_b16 v[220:221], v247 offset:16384\n"
    "ds_read_b64_tr_b16 v[222:223], v247 offset:24576\n"
    "v_mfma_f32_16x16x32_bf16 v[184:187], v[204:207], v[196:199], v[184:187]\n"
    "v_mfma_f32_16x16x32_bf16 v[180:183], v[204:207], v[192:195], v[180:183]\n"
    "s_waitcnt lgkmcnt(6)\n"
    "v_mfma_f32_16x16x32_bf16 v[0:3], v[208:211], v[232:235], v[0:3]\n"
    "ds_read_b64_tr_b16 v[224:225], v248 offset:16384\n"
    "ds_read_b64_tr_b16 v[226:227], v248 offset:24576\n"
    "v_mfma_f32_16x16x32_bf16 v[4:7], v[208:211], v[228:231], v[4:7]\n"
    "v_mfma_f32_16x16x32_bf16 v[8:11], v[208:211], v[236:239], v[8:11]\n"
    "s_waitcnt lgkmcnt(6)\n"
    "v_mfma_f32_16x16x32_bf16 v[20:23], v[212:215], v[236:239], v[20:23]\n"
    "ds_read_b64_tr_b16 v[204:205], v249 offset:16384\n"
    "ds_read_b64_tr_b16 v[206:207], v249 offset:24576\n"
    "v_mfma_f32_16x16x32_bf16 v[16:19], v[212:215], v[228:231], v[16:19]\n"
    "ds_read_b64 v[192:193], v254 offset:12288\n"
    "ds_read_b64 v[194:195], v241 offset:12288\n"
    "v_mfma_f32_16x16x32_bf16 v[12:15], v[212:215], v[232:235], v[12:15]\n"
    "s_waitcnt lgkmcnt(8)\n"
    "v_mfma_f32_16x16x32_bf16 v[24:27], v[216:219], v[232:235], v[24:27]\n"
    "ds_read_b64 v[196:197], v254 offset:14336\n"
    "ds_read_b64 v[198:199], v241 offset:14336\n"
    "v_mfma_f32_16x16x32_bf16 v[28:31], v[216:219], v[228:231], v[28:31]\n"
    "v_mfma_f32_16x16x32_bf16 v[32:35], v[216:219], v[236:239], v[32:35]\n"
    "v_cvt_pk_bf16_f32 v0, v0, v1\n"
    "v_cvt_pk_bf16_f32 v1, v2, v3\n"
    "s_waitcnt lgkmcnt(8)\n"
    "v_mfma_f32_16x16x32_bf16 v[44:47], v[220:223], v[236:239], v[44:47]\n"
    "ds_read_b64 v[200:201], v254 offset:16384\n"
    "ds_read_b64 v[202:203], v241 offset:16384\n"
    "v_mfma_f32_16x16x32_bf16 v[40:43], v[220:223], v[228:231], v[40:43]\n"
    "v_cvt_pk_bf16_f32 v2, v12, v13\n"
    "v_cvt_pk_bf16_f32 v3, v14, v15\n"
    "v_mfma_f32_16x16x32_bf16 v[36:39], v[220:223], v[232:235], v[36:39]\n"
    "ds_read_b64_tr_b16 v[208:209], v250 offset:16384\n"
    "ds_read_b64_tr_b16 v[210:211], v250 offset:24576\n"
    "s_waitcnt lgkmcnt(10)\n"
    "v_mfma_f32_16x16x32_bf16 v[48:51], v[224:227], v[232:235], v[48:51]\n"
    "ds_read_b64_tr_b16 v[212:213], v251 offset:16384\n"
    "ds_read_b64_tr_b16 v[214:215], v251 offset:24576\n"
    "v_mfma_f32_16x16x32_bf16 v[52:55], v[224:227], v[228:231], v[52:55]\n"
    "v_cvt_pk_bf16_f32 v4, v4, v5\n"
    "v_cvt_pk_bf16_f32 v5, v6, v7\n"
    "v_mfma_f32_16x16x32_bf16 v[56:59], v[224:227], v[236:239], v[56:59]\n"
    "v_cvt_pk_bf16_f32 v6, v16, v17\n"
    "v_cvt_pk_bf16_f32 v7, v18, v19\n"
    "s_waitcnt lgkmcnt(10)\n"
    "v_mfma_f32_16x16x32_bf16 v[68:71], v[204:207], v[236:239], v[68:71]\n"
    "ds_read_b64 v[216:217], v242 offset:12288\n"
    "ds_read_b64 v[218:219], v243 offset:12288\n"
    "v_mfma_f32_16x16x32_bf16 v[64:67], v[204:207], v[228:231], v[64:67]\n"
    "ds_read_b64 v[12:13], v242 offset:14336\n"
    "ds_read_b64 v[14:15], v243 offset:14336\n"
    "v_mfma_f32_16x16x32_bf16 v[60:63], v[204:207], v[232:235], v[60:63]\n"
    "v_cvt_pk_bf16_f32 v8, v8, v9\n"
    "v_cvt_pk_bf16_f32 v9, v10, v11\n"
    "v_cvt_pk_bf16_f32 v10, v20, v21\n"
    "v_cvt_pk_bf16_f32 v11, v22, v23\n"
    "s_waitcnt lgkmcnt(12)\n"
    "v_mfma_f32_16x16x32_bf16 v[220:223], v[192:195], v[0:3], 0\n"
    "v_cvt_pk_bf16_f32 v24, v24, v25\n"
    "v_cvt_pk_bf16_f32 v25, v26, v27\n"
    "v_mfma_f32_16x16x32_bf16 v[16:19], v[192:195], v[4:7], 0\n"
    "v_cvt_pk_bf16_f32 v26, v36, v37\n"
    "v_cvt_pk_bf16_f32 v27, v38, v39\n"
    "v_mfma_f32_16x16x32_bf16 v[224:227], v[192:195], v[8:11], 0\n"
    "v_cvt_pk_bf16_f32 v28, v28, v29\n"
    "v_cvt_pk_bf16_f32 v29, v30, v31\n"
    "s_waitcnt lgkmcnt(10)\n"
    "v_mfma_f32_16x16x32_bf16 v[36:39], v[196:199], v[8:11], 0\n"
    "v_cvt_pk_bf16_f32 v30, v40, v41\n"
    "v_cvt_pk_bf16_f32 v31, v42, v43\n"
    "v_mfma_f32_16x16x32_bf16 v[20:23], v[196:199], v[4:7], 0\n"
    "v_cvt_pk_bf16_f32 v32, v32, v33\n"
    "v_cvt_pk_bf16_f32 v33, v34, v35\n"
    "v_mfma_f32_16x16x32_bf16 v[204:207], v[196:199], v[0:3], 0\n"
    "v_cvt_pk_bf16_f32 v34, v44, v45\n"
    "v_cvt_pk_bf16_f32 v35, v46, v47\n"
    "s_waitcnt lgkmcnt(8)\n"
    "v_mfma_f32_16x16x32_bf16 v[192:195], v[200:203], v[0:3], 0\n"
    "ds_read_b64 v[196:197], v242 offset:16384\n"
    "ds_read_b64 v[198:199], v243 offset:16384\n"
    "v_mfma_f32_16x16x32_bf16 v[40:43], v[200:203], v[4:7], 0\n"
    "v_cvt_pk_bf16_f32 v48, v48, v49\n"
    "v_cvt_pk_bf16_f32 v49, v50, v51\n"
    "v_mfma_f32_16x16x32_bf16 v[44:47], v[200:203], v[8:11], 0\n"
    "v_cvt_pk_bf16_f32 v50, v60, v61\n"
    "v_cvt_pk_bf16_f32 v51, v62, v63\n"
    "s_waitcnt lgkmcnt(8)\n"
    "v_mfma_f32_16x16x32_bf16 v[80:83], v[208:211], v[236:239], v[80:83]\n"
    "ds_read_b64_tr_b16 v[60:61], v244 offset:16640\n"
    "ds_read_b64_tr_b16 v[62:63], v244 offset:24832\n"
    "v_mfma_f32_16x16x32_bf16 v[76:79], v[208:211], v[228:231], v[76:79]\n"
    "ds_read_b64_tr_b16 v[200:201], v245 offset:16640\n"
    "ds_read_b64_tr_b16 v[202:203], v245 offset:24832\n"
    "v_mfma_f32_16x16x32_bf16 v[72:75], v[208:211], v[232:235], v[72:75]\n"
    "ds_read_b64 v[0:1], v254 offset:18432\n"
    "ds_read_b64 v[2:3], v241 offset:18432\n"
    "s_waitcnt lgkmcnt(12)\n"
    "v_mfma_f32_16x16x32_bf16 v[84:87], v[212:215], v[232:235], v[84:87]\n"
    "ds_read_b64 v[4:5], v254 offset:20480\n"
    "ds_read_b64 v[6:7], v241 offset:20480\n"
    "v_mfma_f32_16x16x32_bf16 v[88:91], v[212:215], v[228:231], v[88:91]\n"
    "ds_read_b64 v[8:9], v254 offset:22528\n"
    "s_waitcnt lgkmcnt(14)\n"
    "ds_read_b64 v[10:11], v241 offset:22528\n"
    "v_mfma_f32_16x16x32_bf16 v[92:95], v[212:215], v[236:239], v[92:95]\n"
    "s_waitcnt lgkmcnt(14)\n"
    "ds_read_b64_tr_b16 v[208:209], v246 offset:16640\n"
    "s_waitcnt lgkmcnt(14)\n"
    "ds_read_b64_tr_b16 v[210:211], v246 offset:24832\n"
    "v_mfma_f32_16x16x32_bf16 v[224:227], v[216:219], v[32:35], v[224:227]\n"
    "s_waitcnt lgkmcnt(14)\n"
    "ds_read_b64_tr_b16 v[212:213], v247 offset:16640\n"
    "s_waitcnt lgkmcnt(14)\n"
    "ds_read_b64_tr_b16 v[214:215], v247 offset:24832\n"
    "v_mfma_f32_16x16x32_bf16 v[16:19], v[216:219], v[28:31], v[16:19]\n"
    "v_cvt_pk_bf16_f32 v52, v52, v53\n"
    "v_cvt_pk_bf16_f32 v53, v54, v55\n"
    "v_mfma_f32_16x16x32_bf16 v[220:223], v[216:219], v[24:27], v[220:223]\n"
    "v_cvt_pk_bf16_f32 v54, v64, v65\n"
    "v_cvt_pk_bf16_f32 v55, v66, v67\n"
    "s_waitcnt lgkmcnt(0)\n"
    "s_barrier\n"
    "v_mfma_f32_16x16x32_bf16 v[204:207], v[12:15], v[24:27], v[204:207]\n"
    "ds_read_b64 v[64:65], v242 offset:18432\n"
    "ds_read_b64 v[66:67], v243 offset:18432\n"
    "v_mfma_f32_16x16x32_bf16 v[20:23], v[12:15], v[28:31], v[20:23]\n"
    "ds_read_b64 v[216:217], v242 offset:20480\n"
    "ds_read_b64 v[218:219], v243 offset:20480\n"
    "v_mfma_f32_16x16x32_bf16 v[36:39], v[12:15], v[32:35], v[36:39]\n"
    "v_cvt_pk_bf16_f32 v56, v56, v57\n"
    "v_cvt_pk_bf16_f32 v57, v58, v59\n"
    "v_mfma_f32_16x16x32_bf16 v[44:47], v[196:199], v[32:35], v[44:47]\n"
    "ds_read_b64 v[12:13], v242 offset:22528\n"
    "ds_read_b64 v[14:15], v243 offset:22528\n"
    "v_mfma_f32_16x16x32_bf16 v[40:43], v[196:199], v[28:31], v[40:43]\n"
    "v_cvt_pk_bf16_f32 v58, v68, v69\n"
    "v_cvt_pk_bf16_f32 v59, v70, v71\n"
    "v_mfma_f32_16x16x32_bf16 v[192:195], v[196:199], v[24:27], v[192:195]\n"
    "ds_read_b64_tr_b16 v[68:69], v248 offset:16640\n"
    "ds_read_b64_tr_b16 v[70:71], v248 offset:24832\n"
    "v_mfma_f32_16x16x32_bf16 v[96:99], v[60:63], v[232:235], v[96:99]\n"
    "v_cvt_pk_bf16_f32 v72, v72, v73\n"
    "v_cvt_pk_bf16_f32 v73, v74, v75\n"
    "v_mfma_f32_16x16x32_bf16 v[100:103], v[60:63], v[228:231], v[100:103]\n"
    "v_cvt_pk_bf16_f32 v74, v84, v85\n"
    "v_cvt_pk_bf16_f32 v75, v86, v87\n"
    "v_mfma_f32_16x16x32_bf16 v[104:107], v[60:63], v[236:239], v[104:107]\n"
    "v_cvt_pk_bf16_f32 v76, v76, v77\n"
    "v_cvt_pk_bf16_f32 v77, v78, v79\n"
    "v_mfma_f32_16x16x32_bf16 v[116:119], v[200:203], v[236:239], v[116:119]\n"
    "ds_read_b64_tr_b16 v[196:197], v249 offset:16640\n"
    "ds_read_b64_tr_b16 v[198:199], v249 offset:24832\n"
    "v_mfma_f32_16x16x32_bf16 v[112:115], v[200:203], v[228:231], v[112:115]\n"
    "v_cvt_pk_bf16_f32 v78, v88, v89\n"
    "v_cvt_pk_bf16_f32 v79, v90, v91\n"
    "v_mfma_f32_16x16x32_bf16 v[108:111], v[200:203], v[232:235], v[108:111]\n"
    "v_cvt_pk_bf16_f32 v80, v80, v81\n"
    "v_cvt_pk_bf16_f32 v81, v82, v83\n"
    "v_mfma_f32_16x16x32_bf16 v[220:223], v[0:3], v[48:51], v[220:223]\n"
    "v_cvt_pk_bf16_f32 v82, v92, v93\n"
    "v_cvt_pk_bf16_f32 v83, v94, v95\n"
    "v_mfma_f32_16x16x32_bf16 v[16:19], v[0:3], v[52:55], v[16:19]\n"
    "v_mfma_f32_16x16x32_bf16 v[224:227], v[0:3], v[56:59], v[224:227]\n"
    "v_cvt_pk_bf16_f32 v96, v96, v97\n"
    "v_cvt_pk_bf16_f32 v97, v98, v99\n"
    "v_mfma_f32_16x16x32_bf16 v[36:39], v[4:7], v[56:59], v[36:39]\n"
    "v_cvt_pk_bf16_f32 v98, v108, v109\n"
    "v_cvt_pk_bf16_f32 v99, v110, v111\n"
    "v_mfma_f32_16x16x32_bf16 v[20:23], v[4:7], v[52:55], v[20:23]\n"
    "v_cvt_pk_bf16_f32 v100, v100, v101\n"
    "v_cvt_pk_bf16_f32 v101, v102, v103\n"
    "v_mfma_f32_16x16x32_bf16 v[204:207], v[4:7], v[48:51], v[204:207]\n"
    "v_cvt_pk_bf16_f32 v102, v112, v113\n"
    "v_cvt_pk_bf16_f32 v103, v114, v115\n"
    "v_mfma_f32_16x16x32_bf16 v[192:195], v[8:11], v[48:51], v[192:195]\n"
    "v_cvt_pk_bf16_f32 v104, v104, v105\n"
    "v_cvt_pk_bf16_f32 v105, v106, v107\n"
    "v_mfma_f32_16x16x32_bf16 v[40:43], v[8:11], v[52:55], v[40:43]\n"
    "v_cvt_pk_bf16_f32 v106, v116, v117\n"
    "v_cvt_pk_bf16_f32 v107, v118, v119\n"
    "v_mfma_f32_16x16x32_bf16 v[44:47], v[8:11], v[56:59], v[44:47]\n"
    "v_mfma_f32_16x16x32_bf16 v[128:131], v[208:211], v[236:239], v[128:131]\n"
    "v_mfma_f32_16x16x32_bf16 v[124:127], v[208:211], v[228:231], v[124:127]\n"
    "v_mfma_f32_16x16x32_bf16 v[120:123], v[208:211], v[232:235], v[120:123]\n"
    "v_mfma_f32_16x16x32_bf16 v[132:135], v[212:215], v[232:235], v[132:135]\n"
    "v_mfma_f32_16x16x32_bf16 v[136:139], v[212:215], v[228:231], v[136:139]\n"
    "v_mfma_f32_16x16x32_bf16 v[140:143], v[212:215], v[236:239], v[140:143]\n"
    "s_waitcnt lgkmcnt(8)\n"
    "v_mfma_f32_16x16x32_bf16 v[224:227], v[64:67], v[80:83], v[224:227]\n"
    "v_mfma_f32_16x16x32_bf16 v[16:19], v[64:67], v[76:79], v[16:19]\n"
    "v_mfma_f32_16x16x32_bf16 v[220:223], v[64:67], v[72:75], v[220:223]\n"
    "s_nop 0\n"
    "v_cvt_pk_bf16_f32 v120, v120, v121\n"
    "v_cvt_pk_bf16_f32 v121, v122, v123\n"
    "s_waitcnt lgkmcnt(0)\n"
    "s_barrier\n"
    "v_mfma_f32_16x16x32_bf16 v[204:207], v[216:219], v[72:75], v[204:207]\n"
    "ds_read_b64 v[24:25], v254 offset:6144\n"
    "ds_read_b64 v[26:27], v241 offset:6144\n"
    "v_mfma_f32_16x16x32_bf16 v[20:23], v[216:219], v[76:79], v[20:23]\n"
    "ds_read_b64 v[28:29], v254 offset:8192\n"
    "ds_read_b64 v[30:31], v241 offset:8192\n"
    "v_mfma_f32_16x16x32_bf16 v[36:39], v[216:219], v[80:83], v[36:39]\n"
    "ds_read_b64 v[32:33], v254 offset:10240\n"
    "ds_read_b64 v[34:35], v241 offset:10240\n"
    "v_mfma_f32_16x16x32_bf16 v[44:47], v[12:15], v[80:83], v[44:47]\n"
    "ds_read_b64_tr_b16 v[84:85], v250 offset:16640\n"
    "ds_read_b64_tr_b16 v[86:87], v250 offset:24832\n"
    "v_mfma_f32_16x16x32_bf16 v[40:43], v[12:15], v[76:79], v[40:43]\n"
    "ds_read_b64_tr_b16 v[60:61], v251 offset:16640\n"
    "ds_read_b64_tr_b16 v[62:63], v251 offset:24832\n"
    "v_mfma_f32_16x16x32_bf16 v[192:195], v[12:15], v[72:75], v[192:195]\n"
    "ds_read_b64 v[88:89], v242 offset:6144\n"
    "ds_read_b64 v[90:91], v243 offset:6144\n"
    "v_mfma_f32_16x16x32_bf16 v[144:147], v[68:71], v[232:235], v[144:147]\n"
    "ds_read_b64 v[200:201], v242 offset:8192\n"
    "ds_read_b64 v[202:203], v243 offset:8192\n"
    "v_mfma_f32_16x16x32_bf16 v[148:151], v[68:71], v[228:231], v[148:151]\n"
    "ds_read_b64 v[92:93], v242 offset:10240\n"
    "s_waitcnt lgkmcnt(14)\n"
    "ds_read_b64 v[94:95], v243 offset:10240\n"
    "v_mfma_f32_16x16x32_bf16 v[152:155], v[68:71], v[236:239], v[152:155]\n"
    "v_cvt_pk_bf16_f32 v122, v132, v133\n"
    "v_cvt_pk_bf16_f32 v123, v134, v135\n"
    "v_mfma_f32_16x16x32_bf16 v[164:167], v[196:199], v[236:239], v[164:167]\n"
    "v_cvt_pk_bf16_f32 v124, v124, v125\n"
    "v_cvt_pk_bf16_f32 v125, v126, v127\n"
    "v_mfma_f32_16x16x32_bf16 v[160:163], v[196:199], v[228:231], v[160:163]\n"
    "v_cvt_pk_bf16_f32 v126, v136, v137\n"
    "v_cvt_pk_bf16_f32 v127, v138, v139\n"
    "v_mfma_f32_16x16x32_bf16 v[156:159], v[196:199], v[232:235], v[156:159]\n"
    "v_cvt_pk_bf16_f32 v128, v128, v129\n"
    "v_cvt_pk_bf16_f32 v129, v130, v131\n"
    "s_waitcnt lgkmcnt(14)\n"
    "v_mfma_f32_16x16x32_bf16 v[220:223], v[24:27], v[96:99], v[220:223]\n"
    "v_cvt_pk_bf16_f32 v130, v140, v141\n"
    "v_cvt_pk_bf16_f32 v131, v142, v143\n"
    "v_mfma_f32_16x16x32_bf16 v[16:19], v[24:27], v[100:103], v[16:19]\n"
    "v_mfma_f32_16x16x32_bf16 v[224:227], v[24:27], v[104:107], v[224:227]\n"
    "v_cvt_pk_bf16_f32 v144, v144, v145\n"
    "v_cvt_pk_bf16_f32 v145, v146, v147\n"
    "s_waitcnt lgkmcnt(12)\n"
    "v_mfma_f32_16x16x32_bf16 v[36:39], v[28:31], v[104:107], v[36:39]\n"
    "v_cvt_pk_bf16_f32 v146, v156, v157\n"
    "v_cvt_pk_bf16_f32 v147, v158, v159\n"
    "v_mfma_f32_16x16x32_bf16 v[20:23], v[28:31], v[100:103], v[20:23]\n"
    "v_cvt_pk_bf16_f32 v148, v148, v149\n"
    "v_cvt_pk_bf16_f32 v149, v150, v151\n"
    "v_mfma_f32_16x16x32_bf16 v[204:207], v[28:31], v[96:99], v[204:207]\n"
    "v_cvt_pk_bf16_f32 v150, v160, v161\n"
    "v_cvt_pk_bf16_f32 v151, v162, v163\n"
    "s_waitcnt lgkmcnt(10)\n"
    "v_mfma_f32_16x16x32_bf16 v[192:195], v[32:35], v[96:99], v[192:195]\n"
    "v_cvt_pk_bf16_f32 v152, v152, v153\n"
    "v_cvt_pk_bf16_f32 v153, v154, v155\n"
    "v_mfma_f32_16x16x32_bf16 v[40:43], v[32:35], v[100:103], v[40:43]\n"
    "v_cvt_pk_bf16_f32 v154, v164, v165\n"
    "v_cvt_pk_bf16_f32 v155, v166, v167\n"
    "v_mfma_f32_16x16x32_bf16 v[44:47], v[32:35], v[104:107], v[44:47]\n"
    "s_waitcnt lgkmcnt(8)\n"
    "v_mfma_f32_16x16x32_bf16 v[176:179], v[84:87], v[236:239], v[176:179]\n"
    "v_mfma_f32_16x16x32_bf16 v[172:175], v[84:87], v[228:231], v[172:175]\n"
    "v_mfma_f32_16x16x32_bf16 v[168:171], v[84:87], v[232:235], v[168:171]\n"
    "s_waitcnt lgkmcnt(6)\n"
    "v_mfma_f32_16x16x32_bf16 v[180:183], v[60:63], v[232:235], v[180:183]\n"
    "v_mfma_f32_16x16x32_bf16 v[184:187], v[60:63], v[228:231], v[184:187]\n"
    "v_mfma_f32_16x16x32_bf16 v[188:191], v[60:63], v[236:239], v[188:191]\n"
    "s_waitcnt lgkmcnt(4)\n"
    "v_mfma_f32_16x16x32_bf16 v[224:227], v[88:91], v[128:131], v[224:227]\n"
    "v_mfma_f32_16x16x32_bf16 v[16:19], v[88:91], v[124:127], v[16:19]\n"
    "v_mfma_f32_16x16x32_bf16 v[220:223], v[88:91], v[120:123], v[220:223]\n"
    "v_cvt_pk_bf16_f32 v168, v168, v169\n"
    "v_cvt_pk_bf16_f32 v169, v170, v171\n"
    "s_waitcnt lgkmcnt(2)\n"
    "v_mfma_f32_16x16x32_bf16 v[204:207], v[200:203], v[120:123], v[204:207]\n"
    "v_cvt_pk_bf16_f32 v170, v180, v181\n"
    "v_cvt_pk_bf16_f32 v171, v182, v183\n"
    "v_mfma_f32_16x16x32_bf16 v[20:23], v[200:203], v[124:127], v[20:23]\n"
    "v_cvt_pk_bf16_f32 v172, v172, v173\n"
    "v_cvt_pk_bf16_f32 v173, v174, v175\n"
    "v_mfma_f32_16x16x32_bf16 v[36:39], v[200:203], v[128:131], v[36:39]\n"
    "v_cvt_pk_bf16_f32 v174, v184, v185\n"
    "v_cvt_pk_bf16_f32 v175, v186, v187\n"
    "s_waitcnt lgkmcnt(0)\n"
    "s_barrier\n"
    "v_lshrrev_b32 v248, 2, v255\n"
    "v_mul_u32_u24 v248, 192, v248\n"
    "v_lshl_add_u32 v248, v255, 3, v248\n"
    "v_mfma_f32_16x16x32_bf16 v[44:47], v[92:95], v[128:131], v[44:47]\n"
    "ds_read_b64 v[0:1], v254 offset:0\n"
    "ds_read_b64 v[2:3], v241 offset:0\n"
    "v_mfma_f32_16x16x32_bf16 v[40:43], v[92:95], v[124:127], v[40:43]\n"
    "ds_read_b64 v[108:109], v254 offset:2048\n"
    "ds_read_b64 v[110:111], v241 offset:2048\n"
    "v_mfma_f32_16x16x32_bf16 v[192:195], v[92:95], v[120:123], v[192:195]\n"
    "ds_read_b64 v[112:113], v254 offset:4096\n"
    "ds_read_b64 v[114:115], v241 offset:4096\n"
    "s_waitcnt lgkmcnt(4)\n"
    "v_mfma_f32_16x16x32_bf16 v[220:223], v[0:3], v[144:147], v[220:223]\n"
    "ds_read_b64 v[4:5], v242 offset:0\n"
    "ds_read_b64 v[6:7], v243 offset:0\n"
    "v_mfma_f32_16x16x32_bf16 v[16:19], v[0:3], v[148:151], v[16:19]\n"
    "ds_read_b64 v[116:117], v242 offset:2048\n"
    "ds_read_b64 v[118:119], v243 offset:2048\n"
    "v_mfma_f32_16x16x32_bf16 v[224:227], v[0:3], v[152:155], v[224:227]\n"
    "ds_read_b64 v[8:9], v242 offset:4096\n"
    "ds_read_b64 v[10:11], v243 offset:4096\n"
    "s_waitcnt lgkmcnt(8)\n"
    "v_mfma_f32_16x16x32_bf16 v[36:39], v[108:111], v[152:155], v[36:39]\n"
    "v_cvt_pk_bf16_f32 v176, v176, v177\n"
    "v_cvt_pk_bf16_f32 v177, v178, v179\n"
    "v_mfma_f32_16x16x32_bf16 v[20:23], v[108:111], v[148:151], v[20:23]\n"
    "v_cvt_pk_bf16_f32 v178, v188, v189\n"
    "v_cvt_pk_bf16_f32 v179, v190, v191\n"
    "v_mfma_f32_16x16x32_bf16 v[204:207], v[108:111], v[144:147], v[204:207]\n"
    "s_waitcnt lgkmcnt(6)\n"
    "v_mfma_f32_16x16x32_bf16 v[192:195], v[112:115], v[144:147], v[192:195]\n"
    "v_mfma_f32_16x16x32_bf16 v[40:43], v[112:115], v[148:151], v[40:43]\n"
    "v_mfma_f32_16x16x32_bf16 v[44:47], v[112:115], v[152:155], v[44:47]\n"
    "s_waitcnt lgkmcnt(0)\n"
    "s_barrier\n"
    "v_mfma_f32_16x16x32_bf16 v[224:227], v[4:7], v[176:179], v[224:227]\n"
    "ds_read_b64_tr_b16 v[48:49], v248 offset:32768\n"
    "ds_read_b64_tr_b16 v[50:51], v248 offset:36352\n"
    "v_mfma_f32_16x16x32_bf16 v[16:19], v[4:7], v[172:175], v[16:19]\n"
    "ds_read_b128 v[52:55], v252 offset:12288\n"
    "ds_read_b128 v[56:59], v252 offset:14336\n"
    "v_mfma_f32_16x16x32_bf16 v[220:223], v[4:7], v[168:171], v[220:223]\n"
    "ds_read_b128 v[208:211], v252 offset:16384\n"
    "ds_read_b64_tr_b16 v[212:213], v248 offset:32800\n"
    "v_mfma_f32_16x16x32_bf16 v[204:207], v[116:119], v[168:171], v[204:207]\n"
    "ds_read_b64_tr_b16 v[214:215], v248 offset:36384\n"
    "ds_read_b64_tr_b16 v[64:65], v248 offset:32832\n"
    "v_mfma_f32_16x16x32_bf16 v[20:23], v[116:119], v[172:175], v[20:23]\n"
    "ds_read_b64_tr_b16 v[66:67], v248 offset:36416\n"
    "ds_read_b64_tr_b16 v[216:217], v248 offset:32864\n"
    "v_mfma_f32_16x16x32_bf16 v[36:39], v[116:119], v[176:179], v[36:39]\n"
    "ds_read_b64_tr_b16 v[218:219], v248 offset:36448\n"
    "ds_read_b64_tr_b16 v[12:13], v248 offset:32896\n"
    "v_mfma_f32_16x16x32_bf16 v[44:47], v[8:11], v[176:179], v[44:47]\n"
    "ds_read_b64_tr_b16 v[14:15], v248 offset:36480\n"
    "v_mfma_f32_16x16x32_bf16 v[40:43], v[8:11], v[172:175], v[40:43]\n"
    "v_mfma_f32_16x16x32_bf16 v[192:195], v[8:11], v[168:171], v[192:195]\n"
    "s_waitcnt lgkmcnt(10)\n"
    "v_mfma_f32_16x16x32_bf16 v[80:83], v[52:55], v[48:51], 0\n"
    "s_waitcnt lgkmcnt(9)\n"
    "v_mfma_f32_16x16x32_bf16 v[132:135], v[56:59], v[48:51], 0\n"
    "s_waitcnt lgkmcnt(8)\n"
    "v_mfma_f32_16x16x32_bf16 v[68:71], v[208:211], v[48:51], 0\n"
    "v_max3_f32 v72, v220, v221, v222\n"
    "v_max3_f32 v73, v16, v17, v18\n"
    "s_waitcnt lgkmcnt(6)\n"
    "v_mfma_f32_16x16x32_bf16 v[140:143], v[208:211], v[212:215], 0\n"
    "ds_read_b64_tr_b16 v[24:25], v248 offset:32928\n"
    "ds_read_b64_tr_b16 v[26:27], v248 offset:36512\n"
    "v_mfma_f32_16x16x32_bf16 v[196:199], v[56:59], v[212:215], 0\n"
    "v_max3_f32 v74, v224, v225, v226\n"
    "v_max3_f32 v72, v72, v223, v204\n"
    "v_mfma_f32_16x16x32_bf16 v[136:139], v[52:55], v[212:215], 0\n"
    "v_max3_f32 v73, v73, v19, v20\n"
    "v_max3_f32 v74, v74, v227, v36\n"
    "s_waitcnt lgkmcnt(6)\n"
    "v_mfma_f32_16x16x32_bf16 v[156:159], v[52:55], v[64:67], 0\n"
    "ds_read_b64_tr_b16 v[164:165], v248 offset:32960\n"
    "ds_read_b64_tr_b16 v[166:167], v248 offset:36544\n"
    "v_mfma_f32_16x16x32_bf16 v[160:163], v[56:59], v[64:67], 0\n"
    "v_max3_f32 v72, v72, v205, v206\n"
    "v_max3_f32 v73, v73, v21, v22\n"
    "v_mfma_f32_16x16x32_bf16 v[28:31], v[208:211], v[64:67], 0\n"
    "v_max3_f32 v74, v74, v37, v38\n"
    "v_max3_f32 v72, v72, v207, v192\n"
    "s_waitcnt lgkmcnt(6)\n"
    "v_mfma_f32_16x16x32_bf16 v[100:103], v[208:211], v[216:219], 0\n"
    "ds_read_b64_tr_b16 v[104:105], v248 offset:39936\n"
    "ds_read_b64_tr_b16 v[106:107], v248 offset:43520\n"
    "v_mfma_f32_16x16x32_bf16 v[96:99], v[56:59], v[216:219], 0\n"
    "v_max3_f32 v73, v73, v23, v40\n"
    "v_max3_f32 v74, v74, v39, v44\n"
    "v_mfma_f32_16x16x32_bf16 v[32:35], v[52:55], v[216:219], 0\n"
    "v_max3_f32 v72, v72, v193, v194\n"
    "v_max3_f32 v73, v73, v41, v42\n"
    "s_waitcnt lgkmcnt(6)\n"
    "v_mfma_f32_16x16x32_bf16 v[84:87], v[52:55], v[12:15], 0\n"
    "ds_read_b64_tr_b16 v[228:229], v248 offset:39968\n"
    "ds_read_b64_tr_b16 v[230:231], v248 offset:43552\n"
    "v_mfma_f32_16x16x32_bf16 v[60:63], v[56:59], v[12:15], 0\n"
    "ds_read_b128 v[236:239], v253 offset:12288\n"
    "ds_read_b128 v[88:91], v253 offset:14336\n"
    "v_mfma_f32_16x16x32_bf16 v[232:235], v[208:211], v[12:15], 0\n"
    "ds_read_b128 v[180:183], v253 offset:16384\n"
    "v_max3_f32 v74, v74, v45, v46\n"
    "s_waitcnt lgkmcnt(9)\n"
    "v_mfma_f32_16x16x32_bf16 v[244:247], v[208:211], v[24:27], 0\n"
    "ds_read_b64_tr_b16 v[92:93], v248 offset:40000\n"
    "ds_read_b64_tr_b16 v[94:95], v248 offset:43584\n"
    "v_mfma_f32_16x16x32_bf16 v[200:203], v[56:59], v[24:27], 0\n"
    "v_max_f32 v72, v72, v195\n"
    "v_max_f32 v73, v73, v43\n"
    "v_mfma_f32_16x16x32_bf16 v[184:187], v[52:55], v[24:27], 0\n"
    "v_max_f32 v74, v74, v47\n"
    "v_mov_b32 v76, v72\n"
    "s_waitcnt lgkmcnt(9)\n"
    "v_mfma_f32_16x16x32_bf16 v[120:123], v[52:55], v[164:167], 0\n"
    "ds_read_b64_tr_b16 v[0:1], v248 offset:40032\n"
    "ds_read_b64_tr_b16 v[2:3], v248 offset:43616\n"
    "v_mfma_f32_16x16x32_bf16 v[124:127], v[56:59], v[164:167], 0\n"
    "v_mov_b32 v77, v73\n"
    "v_mov_b32 v78, v74\n"
    "v_mfma_f32_16x16x32_bf16 v[128:131], v[208:211], v[164:167], 0\n"
    "v_permlane16_swap_b32 v72, v76\n"
    "v_permlane16_swap_b32 v73, v77\n"
    "s_waitcnt lgkmcnt(4)\n"
    "v_mfma_f32_16x16x32_bf16 v[68:71], v[180:183], v[104:107], v[68:71]\n"
    "ds_read_b64_tr_b16 v[188:189], v248 offset:40064\n"
    "ds_read_b64_tr_b16 v[190:191], v248 offset:43648\n"
    "v_mfma_f32_16x16x32_bf16 v[132:135], v[88:91], v[104:107], v[132:135]\n"
    "v_permlane16_swap_b32 v74, v78\n"
    "v_max_f32 v72, v72, v76\n"
    "v_mfma_f32_16x16x32_bf16 v[80:83], v[236:239], v[104:107], v[80:83]\n"
    "v_max_f32 v73, v73, v77\n"
    "v_max_f32 v74, v74, v78\n"
    "v_mfma_f32_16x16x32_bf16 v[136:139], v[236:239], v[228:231], v[136:139]\n"
    "ds_read_b64_tr_b16 v[108:109], v248 offset:40096\n"
    "ds_read_b64_tr_b16 v[110:111], v248 offset:43680\n"
    "v_mfma_f32_16x16x32_bf16 v[196:199], v[88:91], v[228:231], v[196:199]\n"
    "v_mov_b32 v76, v72\n"
    "v_mov_b32 v77, v73\n"
    "v_mfma_f32_16x16x32_bf16 v[140:143], v[180:183], v[228:231], v[140:143]\n"
    "v_mov_b32 v78, v74\n"
    "v_permlane32_swap_b32 v72, v76\n"
    "s_waitcnt lgkmcnt(6)\n"
    "v_mfma_f32_16x16x32_bf16 v[28:31], v[180:183], v[92:95], v[28:31]\n"
    "ds_read_b64_tr_b16 v[112:113], v248 offset:40128\n"
    "ds_read_b64_tr_b16 v[114:115], v248 offset:43712\n"
    "v_mfma_f32_16x16x32_bf16 v[160:163], v[88:91], v[92:95], v[160:163]\n"
    "v_permlane32_swap_b32 v73, v77\n"
    "v_permlane32_swap_b32 v74, v78\n"
    "v_mfma_f32_16x16x32_bf16 v[156:159], v[236:239], v[92:95], v[156:159]\n"
    "v_max_f32 v72, v72, v76\n"
    "v_max_f32 v73, v73, v77\n"
    "s_waitcnt lgkmcnt(6)\n"
    "v_mfma_f32_16x16x32_bf16 v[32:35], v[236:239], v[0:3], v[32:35]\n"
    "v_max_f32 v74, v74, v78\n"
    "v_mul_f32 v72, s65, v72\n"
    "v_mfma_f32_16x16x32_bf16 v[96:99], v[88:91], v[0:3], v[96:99]\n"
    "v_mul_f32 v73, s65, v73\n"
    "v_mul_f32 v74, s65, v74\n"
    "v_mfma_f32_16x16x32_bf16 v[100:103], v[180:183], v[0:3], v[100:103]\n"
    "v_fma_f32 v220, v220, s65, -v72\n"
    "v_fma_f32 v16, v16, s65, -v73\n"
    "s_waitcnt lgkmcnt(0)\n"
    "s_barrier\n"
    "v_mfma_f32_16x16x32_bf16 v[232:235], v[180:183], v[188:191], v[232:235]\n"
    "ds_read_b64_tr_b16 v[144:145], v248 offset:49152\n"
    "ds_read_b64_tr_b16 v[146:147], v248 offset:52736\n"
    "ds_read_b128 v[148:151], v252 offset:18432\n"
    "ds_read_b128 v[152:155], v252 offset:20480\n"
    "ds_read_b128 v[4:7], v252 offset:22528\n"
    "ds_read_b64_tr_b16 v[116:117], v248 offset:49184\n"
    "ds_read_b64_tr_b16 v[118:119], v248 offset:52768\n"
    "v_fma_f32 v224, v224, s65, -v74\n"
    "v_mfma_f32_16x16x32_bf16 v[60:63], v[88:91], v[188:191], v[60:63]\n"
    "v_exp_f32 v220, v220\n"
    "v_exp_f32 v16, v16\n"
    "v_exp_f32 v224, v224\n"
    "v_fma_f32 v221, v221, s65, -v72\n"
    "v_fma_f32 v17, v17, s65, -v73\n"
    "v_mfma_f32_16x16x32_bf16 v[84:87], v[236:239], v[188:191], v[84:87]\n"
    "v_fma_f32 v225, v225, s65, -v74\n"
    "v_exp_f32 v221, v221\n"
    "v_exp_f32 v17, v17\n"
    "v_exp_f32 v225, v225\n"
    "v_fma_f32 v222, v222, s65, -v72\n"
    "v_mfma_f32_16x16x32_bf16 v[184:187], v[236:239], v[108:111], v[184:187]\n"
    "ds_read_b64_tr_b16 v[8:9], v248 offset:49216\n"
    "ds_read_b64_tr_b16 v[10:11], v248 offset:52800\n"
    "v_fma_f32 v18, v18, s65, -v73\n"
    "v_fma_f32 v226, v226, s65, -v74\n"
    "v_exp_f32 v222, v222\n"
    "v_exp_f32 v18, v18\n"
    "v_mfma_f32_16x16x32_bf16 v[200:203], v[88:91], v[108:111], v[200:203]\n"
    "v_exp_f32 v226, v226\n"
    "v_fma_f32 v223, v223, s65, -v72\n"
    "v_fma_f32 v19, v19, s65, -v73\n"
    "v_fma_f32 v227, v227, s65, -v74\n"
    "v_exp_f32 v223, v223\n"
    "v_exp_f32 v19, v19\n"
    "v_mfma_f32_16x16x32_bf16 v[244:247], v[180:183], v[108:111], v[244:247]\n"
    "v_exp_f32 v227, v227\n"
    "v_fma_f32 v204, v204, s65, -v72\n"
    "v_fma_f32 v20, v20, s65, -v73\n"
    "v_fma_f32 v36, v36, s65, -v74\n"
    "v_exp_f32 v204, v204\n"
    "v_exp_f32 v20, v20\n"
    "v_mfma_f32_16x16x32_bf16 v[128:131], v[180:183], v[112:115], v[128:131]\n"
    "ds_read_b64_tr_b16 v[168:169], v248 offset:49248\n"
    "ds_read_b64_tr_b16 v[170:171], v248 offset:52832\n"
    "v_exp_f32 v36, v36\n"
    "v_fma_f32 v205, v205, s65, -v72\n"
    "v_fma_f32 v21, v21, s65, -v73\n"
    "v_fma_f32 v37, v37, s65, -v74\n"
    "v_exp_f32 v205, v205\n"
    "v_mfma_f32_16x16x32_bf16 v[124:127], v[88:91], v[112:115], v[124:127]\n"
    "v_exp_f32 v21, v21\n"
    "v_exp_f32 v37, v37\n"
    "v_fma_f32 v206, v206, s65, -v72\n"
    "v_fma_f32 v22, v22, s65, -v73\n"
    "v_fma_f32 v38, v38, s65, -v74\n"
    "v_exp_f32 v206, v206\n"
    "v_mfma_f32_16x16x32_bf16 v[120:123], v[236:239], v[112:115], v[120:123]\n"
    "v_exp_f32 v22, v22\n"
    "v_exp_f32 v38, v38\n"
    "v_fma_f32 v207, v207, s65, -v72\n"
    "v_fma_f32 v23, v23, s65, -v73\n"
    "v_fma_f32 v39, v39, s65, -v74\n"
    "v_exp_f32 v207, v207\n"
    "s_waitcnt lgkmcnt(8)\n"
    "v_mfma_f32_16x16x32_bf16 v[80:83], v[148:151], v[144:147], v[80:83]\n"
    "ds_read_b64_tr_b16 v[172:173], v248 offset:49280\n"
    "ds_read_b64_tr_b16 v[174:175], v248 offset:52864\n"
    "v_exp_f32 v23, v23\n"
    "v_exp_f32 v39, v39\n"
    "v_fma_f32 v192, v192, s65, -v72\n"
    "v_fma_f32 v40, v40, s65, -v73\n"
    "s_waitcnt lgkmcnt(9)\n"
    "v_mfma_f32_16x16x32_bf16 v[132:135], v[152:155], v[144:147], v[132:135]\n"
    "v_fma_f32 v44, v44, s65, -v74\n"
    "v_exp_f32 v192, v192\n"
    "v_exp_f32 v40, v40\n"
    "v_exp_f32 v44, v44\n"
    "v_fma_f32 v193, v193, s65, -v72\n"
    "s_waitcnt lgkmcnt(8)\n"
    "v_mfma_f32_16x16x32_bf16 v[68:71], v[4:7], v[144:147], v[68:71]\n"
    "v_fma_f32 v41, v41, s65, -v73\n"
    "v_fma_f32 v45, v45, s65, -v74\n"
    "v_exp_f32 v193, v193\n"
    "v_exp_f32 v41, v41\n"
    "v_exp_f32 v45, v45\n"
    "s_waitcnt lgkmcnt(6)\n"
    "v_mfma_f32_16x16x32_bf16 v[140:143], v[4:7], v[116:119], v[140:143]\n"
    "ds_read_b64_tr_b16 v[176:177], v248 offset:49312\n"
    "ds_read_b64_tr_b16 v[178:179], v248 offset:52896\n"
    "v_fma_f32 v194, v194, s65, -v72\n"
    "v_fma_f32 v42, v42, s65, -v73\n"
    "v_fma_f32 v46, v46, s65, -v74\n"
    "v_exp_f32 v194, v194\n"
    "v_exp_f32 v42, v42\n"
    "v_mfma_f32_16x16x32_bf16 v[196:199], v[152:155], v[116:119], v[196:199]\n"
    "v_exp_f32 v46, v46\n"
    "v_fma_f32 v195, v195, s65, -v72\n"
    "v_fma_f32 v43, v43, s65, -v73\n"
    "v_fma_f32 v47, v47, s65, -v74\n"
    "v_exp_f32 v195, v195\n"
    "v_exp_f32 v43, v43\n"
    "v_mfma_f32_16x16x32_bf16 v[136:139], v[148:151], v[116:119], v[136:139]\n"
    "v_exp_f32 v47, v47\n"
    "v_add_f32 v72, v220, v221\n"
    "v_add_f32 v73, v16, v17\n"
    "v_add_f32 v74, v224, v225\n"
    "v_add_f32 v72, v72, v222\n"
    "v_add_f32 v73, v73, v18\n"
    "v_add_f32 v74, v74, v226\n"
    "s_waitcnt lgkmcnt(6)\n"
    "v_mfma_f32_16x16x32_bf16 v[156:159], v[148:151], v[8:11], v[156:159]\n"
    "ds_read_b64_tr_b16 v[240:241], v248 offset:49344\n"
    "ds_read_b64_tr_b16 v[242:243], v248 offset:52928\n"
    "v_add_f32 v72, v72, v223\n"
    "v_add_f32 v73, v73, v19\n"
    "v_add_f32 v74, v74, v227\n"
    "v_add_f32 v72, v72, v204\n"
    "v_add_f32 v73, v73, v20\n"
    "v_add_f32 v74, v74, v36\n"
    "v_mfma_f32_16x16x32_bf16 v[160:163], v[152:155], v[8:11], v[160:163]\n"
    "v_add_f32 v72, v72, v205\n"
    "v_add_f32 v73, v73, v21\n"
    "v_add_f32 v74, v74, v37\n"
    "v_add_f32 v72, v72, v206\n"
    "v_add_f32 v73, v73, v22\n"
    "v_add_f32 v74, v74, v38\n"
    "v_add_f32 v72, v72, v207\n"
    "v_add_f32 v73, v73, v23\n"
    "v_mfma_f32_16x16x32_bf16 v[28:31], v[4:7], v[8:11], v[28:31]\n"
    "v_add_f32 v74, v74, v39\n"
    "v_add_f32 v72, v72, v192\n"
    "v_add_f32 v73, v73, v40\n"
    "v_add_f32 v74, v74, v44\n"
    "v_add_f32 v72, v72, v193\n"
    "v_add_f32 v73, v73, v41\n"
    "v_add_f32 v74, v74, v45\n"
    "v_add_f32 v72, v72, v194\n"
    "s_waitcnt lgkmcnt(6)\n"
    "v_mfma_f32_16x16x32_bf16 v[100:103], v[4:7], v[168:171], v[100:103]\n"
    "ds_read_b64_tr_b16 v[48:49], v248 offset:56320\n"
    "ds_read_b64_tr_b16 v[50:51], v248 offset:59904\n"
    "v_add_f32 v73, v73, v42\n"
    "v_add_f32 v74, v74, v46\n"
    "v_add_f32 v72, v72, v195\n"
    "v_add_f32 v73, v73, v43\n"
    "v_add_f32 v74, v74, v47\n"
    "v_mov_b32 v76, v72\n"
    "v_mfma_f32_16x16x32_bf16 v[96:99], v[152:155], v[168:171], v[96:99]\n"
    "v_mov_b32 v77, v73\n"
    "v_mov_b32 v78, v74\n"
    "v_permlane16_swap_b32 v72, v76\n"
    "v_permlane16_swap_b32 v73, v77\n"
    "v_permlane16_swap_b32 v74, v78\n"
    "v_add_f32 v72, v72, v76\n"
    "v_add_f32 v73, v73, v77\n"
    "v_add_f32 v74, v74, v78\n"
    "v_mfma_f32_16x16x32_bf16 v[32:35], v[148:151], v[168:171], v[32:35]\n"
    "v_mov_b32 v76, v72\n"
    "v_mov_b32 v77, v73\n"
    "v_mov_b32 v78, v74\n"
    "v_permlane32_swap_b32 v72, v76\n"
    "v_permlane32_swap_b32 v73, v77\n"
    "v_permlane32_swap_b32 v74, v78\n"
    "v_add_f32 v72, v72, v76\n"
    "v_add_f32 v73, v73, v77\n"
    "s_waitcnt lgkmcnt(6)\n"
    "v_mfma_f32_16x16x32_bf16 v[84:87], v[148:151], v[172:175], v[84:87]\n"
    "ds_read_b64_tr_b16 v[212:213], v248 offset:56352\n"
    "ds_read_b64_tr_b16 v[214:215], v248 offset:59936\n"
    "ds_read_b128 v[64:67], v253 offset:18432\n"
    "ds_read_b128 v[216:219], v253 offset:20480\n"
    "ds_read_b128 v[12:15], v253 offset:22528\n"
    "v_add_f32 v74, v74, v78\n"
    "v_rcp_f32 v72, v72\n"
    "v_mfma_f32_16x16x32_bf16 v[60:63], v[152:155], v[172:175], v[60:63]\n"
    "v_rcp_f32 v73, v73\n"
    "v_rcp_f32 v74, v74\n"
    "v_lshlrev_b32 v76, 4, v255\n"
    "v_lshlrev_b32 v78, 3, v255\n"
    "v_lshl_add_u32 v250, v255, 3, s56\n"
    "v_lshl_add_u32 v249, v255, 4, s56\n"
    "v_mfma_f32_16x16x32_bf16 v[232:235], v[4:7], v[172:175], v[232:235]\n"
    "v_mul_f32 v220, v220, v72\n"
    "v_mul_f32 v16, v16, v73\n"
    "v_mul_f32 v224, v224, v74\n"
    "v_mul_f32 v221, v221, v72\n"
    "v_mul_f32 v17, v17, v73\n"
    "v_mul_f32 v225, v225, v74\n"
    "v_mul_f32 v222, v222, v72\n"
    "v_mul_f32 v18, v18, v73\n"
    "s_waitcnt lgkmcnt(9)\n"
    "v_mfma_f32_16x16x32_bf16 v[244:247], v[4:7], v[176:179], v[244:247]\n"
    "ds_read_b64_tr_b16 v[24:25], v248 offset:56384\n"
    "ds_read_b64_tr_b16 v[26:27], v248 offset:59968\n"
    "v_mul_f32 v226, v226, v74\n"
    "v_mul_f32 v223, v223, v72\n"
    "v_mul_f32 v19, v19, v73\n"
    "v_mul_f32 v227, v227, v74\n"
    "v_cvt_pk_bf16_f32 v220, v220, v221\n"
    "v_cvt_pk_bf16_f32 v221, v222, v223\n"
    "v_mfma_f32_16x16x32_bf16 v[200:203], v[152:155], v[176:179], v[200:203]\n"
    "v_cvt_pk_bf16_f32 v16, v16, v17\n"
    "v_cvt_pk_bf16_f32 v17, v18, v19\n"
    "v_cvt_pk_bf16_f32 v224, v224, v225\n"
    "v_cvt_pk_bf16_f32 v225, v226, v227\n"
    "v_mul_f32 v204, v204, v72\n"
    "v_mul_f32 v20, v20, v73\n"
    "v_mul_f32 v36, v36, v74\n"
    "v_mul_f32 v205, v205, v72\n"
    "v_mfma_f32_16x16x32_bf16 v[184:187], v[148:151], v[176:179], v[184:187]\n"
    "v_mul_f32 v21, v21, v73\n"
    "v_mul_f32 v37, v37, v74\n"
    "v_mul_f32 v206, v206, v72\n"
    "v_mul_f32 v22, v22, v73\n"
    "v_mul_f32 v38, v38, v74\n"
    "v_mul_f32 v207, v207, v72\n"
    "v_mul_f32 v23, v23, v73\n"
    "v_mul_f32 v39, v39, v74\n"
    "s_waitcnt lgkmcnt(9)\n"
    "v_mfma_f32_16x16x32_bf16 v[120:123], v[148:151], v[240:243], v[120:123]\n"
    "ds_read_b64_tr_b16 v[164:165], v248 offset:56416\n"
    "ds_read_b64_tr_b16 v[166:167], v248 offset:60000\n"
    "v_cvt_pk_bf16_f32 v222, v204, v205\n"
    "v_cvt_pk_bf16_f32 v223, v206, v207\n"
    "v_cvt_pk_bf16_f32 v18, v20, v21\n"
    "v_cvt_pk_bf16_f32 v19, v22, v23\n"
    "v_cvt_pk_bf16_f32 v226, v36, v37\n"
    "v_cvt_pk_bf16_f32 v227, v38, v39\n"
    "v_mfma_f32_16x16x32_bf16 v[124:127], v[152:155], v[240:243], v[124:127]\n"
    "v_mul_f32 v192, v192, v72\n"
    "v_mul_f32 v40, v40, v73\n"
    "v_mul_f32 v44, v44, v74\n"
    "v_mul_f32 v193, v193, v72\n"
    "v_mul_f32 v41, v41, v73\n"
    "v_mul_f32 v45, v45, v74\n"
    "v_mul_f32 v194, v194, v72\n"
    "v_mul_f32 v42, v42, v73\n"
    "v_mfma_f32_16x16x32_bf16 v[128:131], v[4:7], v[240:243], v[128:131]\n"
    "v_mul_f32 v46, v46, v74\n"
    "v_mul_f32 v195, v195, v72\n"
    "v_mul_f32 v43, v43, v73\n"
    "v_mul_f32 v47, v47, v74\n"
    "v_cvt_pk_bf16_f32 v192, v192, v193\n"
    "v_cvt_pk_bf16_f32 v193, v194, v195\n"
    "v_cvt_pk_bf16_f32 v194, v40, v41\n"
    "v_cvt_pk_bf16_f32 v195, v42, v43\n"
    "s_waitcnt lgkmcnt(4)\n"
    "v_mfma_f32_16x16x32_bf16 v[68:71], v[12:15], v[48:51], v[68:71]\n"
    "ds_read_b64_tr_b16 v[52:53], v248 offset:56448\n"
    "ds_read_b64_tr_b16 v[54:55], v248 offset:60032\n"
    "v_cvt_pk_bf16_f32 v44, v44, v45\n"
    "v_cvt_pk_bf16_f32 v45, v46, v47\n"
    "buffer_store_dwordx4 v[220:223], v76, s[36:39], s58 offen offset:0 sc1\n"
    "buffer_store_dwordx4 v[16:19], v76, s[36:39], s58 offen offset:1024 sc1\n"
    "v_mfma_f32_16x16x32_bf16 v[132:135], v[216:219], v[48:51], v[132:135]\n"
    "buffer_store_dwordx4 v[224:227], v76, s[36:39], s58 offen offset:2048 sc1\n"
    "v_mfma_f32_16x16x32_bf16 v[80:83], v[64:67], v[48:51], v[80:83]\n"
    "buffer_store_dwordx4 v[192:195], v76, s[36:39], s77 offen offset:0 sc1\n"
    "v_mfma_f32_16x16x32_bf16 v[136:139], v[64:67], v[212:215], v[136:139]\n"
    "ds_read_b64_tr_b16 v[56:57], v248 offset:56480\n"
    "ds_read_b64_tr_b16 v[58:59], v248 offset:60064\n"
    "v_mfma_f32_16x16x32_bf16 v[196:199], v[216:219], v[212:215], v[196:199]\n"
    "buffer_store_dwordx2 v[44:45], v78, s[36:39], s77 offen offset:1024 sc1\n"
    "v_mfma_f32_16x16x32_bf16 v[140:143], v[12:15], v[212:215], v[140:143]\n"
    "s_waitcnt lgkmcnt(6)\n"
    "v_mfma_f32_16x16x32_bf16 v[28:31], v[12:15], v[24:27], v[28:31]\n"
    "ds_read_b64_tr_b16 v[208:209], v248 offset:56512\n"
    "ds_read_b64_tr_b16 v[210:211], v248 offset:60096\n"
    "v_mfma_f32_16x16x32_bf16 v[160:163], v[216:219], v[24:27], v[160:163]\n"
    "v_mfma_f32_16x16x32_bf16 v[156:159], v[64:67], v[24:27], v[156:159]\n"
    "s_waitcnt lgkmcnt(6)\n"
    "v_mfma_f32_16x16x32_bf16 v[32:35], v[64:67], v[164:167], v[32:35]\n"
    "v_mfma_f32_16x16x32_bf16 v[96:99], v[216:219], v[164:167], v[96:99]\n"
    "v_mfma_f32_16x16x32_bf16 v[100:103], v[12:15], v[164:167], v[100:103]\n"
    "s_waitcnt lgkmcnt(0)\n"
    "s_barrier\n"
    "v_mfma_f32_16x16x32_bf16 v[232:235], v[12:15], v[52:55], v[232:235]\n"
    "ds_read_b64_tr_b16 v[104:105], v248 offset:0\n"
    "ds_read_b64_tr_b16 v[106:107], v248 offset:3584\n"
    "v_mfma_f32_16x16x32_bf16 v[60:63], v[216:219], v[52:55], v[60:63]\n"
    "ds_read_b128 v[228:231], v252 offset:6144\n"
    "ds_read_b128 v[92:95], v252 offset:8192\n"
    "v_mfma_f32_16x16x32_bf16 v[84:87], v[64:67], v[52:55], v[84:87]\n"
    "ds_read_b128 v[0:3], v252 offset:10240\n"
    "ds_read_b64_tr_b16 v[188:189], v248 offset:32\n"
    "v_mfma_f32_16x16x32_bf16 v[184:187], v[64:67], v[56:59], v[184:187]\n"
    "ds_read_b64_tr_b16 v[190:191], v248 offset:3616\n"
    "ds_read_b64_tr_b16 v[108:109], v248 offset:64\n"
    "v_mfma_f32_16x16x32_bf16 v[200:203], v[216:219], v[56:59], v[200:203]\n"
    "ds_read_b64_tr_b16 v[110:111], v248 offset:3648\n"
    "v_mfma_f32_16x16x32_bf16 v[244:247], v[12:15], v[56:59], v[244:247]\n"
    "v_mfma_f32_16x16x32_bf16 v[128:131], v[12:15], v[208:211], v[128:131]\n"
    "ds_read_b64_tr_b16 v[112:113], v248 offset:96\n"
    "ds_read_b64_tr_b16 v[114:115], v248 offset:3680\n"
    "v_mfma_f32_16x16x32_bf16 v[124:127], v[216:219], v[208:211], v[124:127]\n"
    "v_mfma_f32_16x16x32_bf16 v[120:123], v[64:67], v[208:211], v[120:123]\n"
    "s_waitcnt lgkmcnt(8)\n"
    "v_mfma_f32_16x16x32_bf16 v[80:83], v[228:231], v[104:107], v[80:83]\n"
    "ds_read_b64_tr_b16 v[236:237], v248 offset:128\n"
    "ds_read_b64_tr_b16 v[238:239], v248 offset:3712\n"
    "s_waitcnt lgkmcnt(9)\n"
    "v_mfma_f32_16x16x32_bf16 v[132:135], v[92:95], v[104:107], v[132:135]\n"
    "s_waitcnt lgkmcnt(8)\n"
    "v_mfma_f32_16x16x32_bf16 v[68:71], v[0:3], v[104:107], v[68:71]\n"
    "s_waitcnt lgkmcnt(6)\n"
    "v_mfma_f32_16x16x32_bf16 v[140:143], v[0:3], v[188:191], v[140:143]\n"
    "ds_read_b64_tr_b16 v[88:89], v248 offset:160\n"
    "ds_read_b64_tr_b16 v[90:91], v248 offset:3744\n"
    "v_mfma_f32_16x16x32_bf16 v[196:199], v[92:95], v[188:191], v[196:199]\n"
    "v_mfma_f32_16x16x32_bf16 v[136:139], v[228:231], v[188:191], v[136:139]\n"
    "s_waitcnt lgkmcnt(6)\n"
    "v_mfma_f32_16x16x32_bf16 v[156:159], v[228:231], v[108:111], v[156:159]\n"
    "ds_read_b64_tr_b16 v[180:181], v248 offset:192\n"
    "ds_read_b64_tr_b16 v[182:183], v248 offset:3776\n"
    "v_mfma_f32_16x16x32_bf16 v[160:163], v[92:95], v[108:111], v[160:163]\n"
    "v_mfma_f32_16x16x32_bf16 v[28:31], v[0:3], v[108:111], v[28:31]\n"
    "s_waitcnt lgkmcnt(6)\n"
    "v_mfma_f32_16x16x32_bf16 v[100:103], v[0:3], v[112:115], v[100:103]\n"
    "ds_read_b64_tr_b16 v[144:145], v248 offset:7168\n"
    "ds_read_b64_tr_b16 v[146:147], v248 offset:10752\n"
    "v_mfma_f32_16x16x32_bf16 v[96:99], v[92:95], v[112:115], v[96:99]\n"
    "v_mfma_f32_16x16x32_bf16 v[32:35], v[228:231], v[112:115], v[32:35]\n"
    "s_waitcnt lgkmcnt(6)\n"
    "v_mfma_f32_16x16x32_bf16 v[84:87], v[228:231], v[236:239], v[84:87]\n"
    "ds_read_b64_tr_b16 v[116:117], v248 offset:7200\n"
    "ds_read_b64_tr_b16 v[118:119], v248 offset:10784\n"
    "v_mfma_f32_16x16x32_bf16 v[60:63], v[92:95], v[236:239], v[60:63]\n"
    "ds_read_b128 v[8:11], v253 offset:6144\n"
    "ds_read_b128 v[168:171], v253 offset:8192\n"
    "v_mfma_f32_16x16x32_bf16 v[232:235], v[0:3], v[236:239], v[232:235]\n"
    "ds_read_b128 v[172:175], v253 offset:10240\n"
    "s_waitcnt lgkmcnt(9)\n"
    "v_mfma_f32_16x16x32_bf16 v[244:247], v[0:3], v[88:91], v[244:247]\n"
    "ds_read_b64_tr_b16 v[176:177], v248 offset:7232\n"
    "ds_read_b64_tr_b16 v[178:179], v248 offset:10816\n"
    "v_mfma_f32_16x16x32_bf16 v[200:203], v[92:95], v[88:91], v[200:203]\n"
    "v_mfma_f32_16x16x32_bf16 v[184:187], v[228:231], v[88:91], v[184:187]\n"
    "s_waitcnt lgkmcnt(9)\n"
    "v_mfma_f32_16x16x32_bf16 v[120:123], v[228:231], v[180:183], v[120:123]\n"
    "ds_read_b64_tr_b16 v[240:241], v248 offset:7264\n"
    "ds_read_b64_tr_b16 v[242:243], v248 offset:10848\n"
    "v_mfma_f32_16x16x32_bf16 v[124:127], v[92:95], v[180:183], v[124:127]\n"
    "v_mfma_f32_16x16x32_bf16 v[128:131], v[0:3], v[180:183], v[128:131]\n"
    "s_waitcnt lgkmcnt(4)\n"
    "v_mfma_f32_16x16x32_bf16 v[68:71], v[172:175], v[144:147], v[68:71]\n"
    "ds_read_b64_tr_b16 v[148:149], v248 offset:7296\n"
    "ds_read_b64_tr_b16 v[150:151], v248 offset:10880\n"
    "v_mfma_f32_16x16x32_bf16 v[132:135], v[168:171], v[144:147], v[132:135]\n"
    "v_mfma_f32_16x16x32_bf16 v[80:83], v[8:11], v[144:147], v[80:83]\n"
    "v_mfma_f32_16x16x32_bf16 v[136:139], v[8:11], v[116:119], v[136:139]\n"
    "ds_read_b64_tr_b16 v[152:153], v248 offset:7328\n"
    "ds_read_b64_tr_b16 v[154:155], v248 offset:10912\n"
    "v_mfma_f32_16x16x32_bf16 v[196:199], v[168:171], v[116:119], v[196:199]\n"
    "v_mfma_f32_16x16x32_bf16 v[140:143], v[172:175], v[116:119], v[140:143]\n"
    "s_waitcnt lgkmcnt(6)\n"
    "v_mfma_f32_16x16x32_bf16 v[28:31], v[172:175], v[176:179], v[28:31]\n"
    "ds_read_b64_tr_b16 v[4:5], v248 offset:7360\n"
    "ds_read_b64_tr_b16 v[6:7], v248 offset:10944\n"
    "v_mfma_f32_16x16x32_bf16 v[160:163], v[168:171], v[176:179], v[160:163]\n"
    "v_mfma_f32_16x16x32_bf16 v[156:159], v[8:11], v[176:179], v[156:159]\n"
    "s_waitcnt lgkmcnt(6)\n"
    "v_mfma_f32_16x16x32_bf16 v[32:35], v[8:11], v[240:243], v[32:35]\n"
    "v_mfma_f32_16x16x32_bf16 v[96:99], v[168:171], v[240:243], v[96:99]\n"
    "v_mfma_f32_16x16x32_bf16 v[100:103], v[172:175], v[240:243], v[100:103]\n"
    "s_waitcnt lgkmcnt(0)\n"
    "s_barrier\n"
    "v_mfma_f32_16x16x32_bf16 v[232:235], v[172:175], v[148:151], v[232:235]\n"
    "ds_read_b64_tr_b16 v[48:49], v248 offset:16384\n"
    "ds_read_b64_tr_b16 v[50:51], v248 offset:19968\n"
    "v_mfma_f32_16x16x32_bf16 v[60:63], v[168:171], v[148:151], v[60:63]\n"
    "ds_read_b128 v[220:223], v252 offset:0\n"
    "ds_read_b128 v[16:19], v252 offset:2048\n"
    "v_mfma_f32_16x16x32_bf16 v[84:87], v[8:11], v[148:151], v[84:87]\n"
    "ds_read_b128 v[224:227], v252 offset:4096\n"
    "ds_read_b64_tr_b16 v[204:205], v248 offset:16416\n"
    "v_mfma_f32_16x16x32_bf16 v[184:187], v[8:11], v[152:155], v[184:187]\n"
    "ds_read_b64_tr_b16 v[206:207], v248 offset:20000\n"
    "ds_read_b64_tr_b16 v[20:21], v248 offset:16448\n"
    "v_mfma_f32_16x16x32_bf16 v[200:203], v[168:171], v[152:155], v[200:203]\n"
    "ds_read_b64_tr_b16 v[22:23], v248 offset:20032\n"
    "v_mfma_f32_16x16x32_bf16 v[244:247], v[172:175], v[152:155], v[244:247]\n"
    "v_mfma_f32_16x16x32_bf16 v[128:131], v[172:175], v[4:7], v[128:131]\n"
    "ds_read_b64_tr_b16 v[36:37], v248 offset:16480\n"
    "ds_read_b64_tr_b16 v[38:39], v248 offset:20064\n"
    "v_mfma_f32_16x16x32_bf16 v[124:127], v[168:171], v[4:7], v[124:127]\n"
    "v_mfma_f32_16x16x32_bf16 v[120:123], v[8:11], v[4:7], v[120:123]\n"
    "s_waitcnt lgkmcnt(8)\n"
    "v_mfma_f32_16x16x32_bf16 v[80:83], v[220:223], v[48:51], v[80:83]\n"
    "ds_read_b64_tr_b16 v[192:193], v248 offset:16512\n"
    "ds_read_b64_tr_b16 v[194:195], v248 offset:20096\n"
    "s_waitcnt lgkmcnt(9)\n"
    "v_mfma_f32_16x16x32_bf16 v[132:135], v[16:19], v[48:51], v[132:135]\n"
    "s_waitcnt lgkmcnt(8)\n"
    "v_mfma_f32_16x16x32_bf16 v[68:71], v[224:227], v[48:51], v[68:71]\n"
    "s_waitcnt lgkmcnt(6)\n"
    "v_mfma_f32_16x16x32_bf16 v[140:143], v[224:227], v[204:207], v[140:143]\n"
    "ds_read_b64_tr_b16 v[40:41], v248 offset:16544\n"
    "ds_read_b64_tr_b16 v[42:43], v248 offset:20128\n"
    "v_mfma_f32_16x16x32_bf16 v[196:199], v[16:19], v[204:207], v[196:199]\n"
    "v_mfma_f32_16x16x32_bf16 v[136:139], v[220:223], v[204:207], v[136:139]\n"
    "s_waitcnt lgkmcnt(6)\n"
    "v_mfma_f32_16x16x32_bf16 v[156:159], v[220:223], v[20:23], v[156:159]\n"
    "ds_read_b64_tr_b16 v[44:45], v248 offset:16576\n"
    "ds_read_b64_tr_b16 v[46:47], v248 offset:20160\n"
    "v_mfma_f32_16x16x32_bf16 v[160:163], v[16:19], v[20:23], v[160:163]\n"
    "v_mfma_f32_16x16x32_bf16 v[28:31], v[224:227], v[20:23], v[28:31]\n"
    "s_waitcnt lgkmcnt(6)\n"
    "v_mfma_f32_16x16x32_bf16 v[100:103], v[224:227], v[36:39], v[100:103]\n"
    "ds_read_b64_tr_b16 v[72:73], v248 offset:23552\n"
    "ds_read_b64_tr_b16 v[74:75], v248 offset:27136\n"
    "v_mfma_f32_16x16x32_bf16 v[96:99], v[16:19], v[36:39], v[96:99]\n"
    "v_mfma_f32_16x16x32_bf16 v[32:35], v[220:223], v[36:39], v[32:35]\n"
    "s_waitcnt lgkmcnt(6)\n"
    "v_mfma_f32_16x16x32_bf16 v[84:87], v[220:223], v[192:195], v[84:87]\n"
    "ds_read_b64_tr_b16 v[76:77], v248 offset:23584\n"
    "ds_read_b64_tr_b16 v[78:79], v248 offset:27168\n"
    "v_mfma_f32_16x16x32_bf16 v[60:63], v[16:19], v[192:195], v[60:63]\n"
    "ds_read_b128 v[212:215], v253 offset:0\n"
    "ds_read_b128 v[24:27], v253 offset:2048\n"
    "v_mfma_f32_16x16x32_bf16 v[232:235], v[224:227], v[192:195], v[232:235]\n"
    "ds_read_b128 v[164:167], v253 offset:4096\n"
    "s_waitcnt lgkmcnt(9)\n"
    "v_mfma_f32_16x16x32_bf16 v[244:247], v[224:227], v[40:43], v[244:247]\n"
    "ds_read_b64_tr_b16 v[52:53], v248 offset:23616\n"
    "ds_read_b64_tr_b16 v[54:55], v248 offset:27200\n"
    "v_mfma_f32_16x16x32_bf16 v[200:203], v[16:19], v[40:43], v[200:203]\n"
    "v_mfma_f32_16x16x32_bf16 v[184:187], v[220:223], v[40:43], v[184:187]\n"
    "s_waitcnt lgkmcnt(9)\n"
    "v_mfma_f32_16x16x32_bf16 v[120:123], v[220:223], v[44:47], v[120:123]\n"
    "ds_read_b64_tr_b16 v[56:57], v248 offset:23648\n"
    "ds_read_b64_tr_b16 v[58:59], v248 offset:27232\n"
    "v_mfma_f32_16x16x32_bf16 v[124:127], v[16:19], v[44:47], v[124:127]\n"
    "v_mfma_f32_16x16x32_bf16 v[128:131], v[224:227], v[44:47], v[128:131]\n"
    "s_waitcnt lgkmcnt(4)\n"
    "v_mfma_f32_16x16x32_bf16 v[68:71], v[164:167], v[72:75], v[68:71]\n"
    "ds_read_b64_tr_b16 v[208:209], v248 offset:23680\n"
    "ds_read_b64_tr_b16 v[210:211], v248 offset:27264\n"
    "v_mfma_f32_16x16x32_bf16 v[132:135], v[24:27], v[72:75], v[132:135]\n"
    "v_mfma_f32_16x16x32_bf16 v[80:83], v[212:215], v[72:75], v[80:83]\n"
    "v_mfma_f32_16x16x32_bf16 v[136:139], v[212:215], v[76:79], v[136:139]\n"
    "ds_read_b64_tr_b16 v[64:65], v248 offset:23712\n"
    "ds_read_b64_tr_b16 v[66:67], v248 offset:27296\n"
    "v_mfma_f32_16x16x32_bf16 v[196:199], v[24:27], v[76:79], v[196:199]\n"
    "v_mfma_f32_16x16x32_bf16 v[140:143], v[164:167], v[76:79], v[140:143]\n"
    "s_nop 2\n"
    "v_cvt_pk_bf16_f32 v80, v80, v81\n"
    "v_cvt_pk_bf16_f32 v81, v82, v83\n"
    "s_waitcnt lgkmcnt(6)\n"
    "v_mfma_f32_16x16x32_bf16 v[28:31], v[164:167], v[52:55], v[28:31]\n"
    "ds_read_b64_tr_b16 v[216:217], v248 offset:23744\n"
    "ds_read_b64_tr_b16 v[218:219], v248 offset:27328\n"
    "v_mfma_f32_16x16x32_bf16 v[160:163], v[24:27], v[52:55], v[160:163]\n"
    "v_cvt_pk_bf16_f32 v82, v132, v133\n"
    "v_cvt_pk_bf16_f32 v83, v134, v135\n"
    "v_mfma_f32_16x16x32_bf16 v[156:159], v[212:215], v[52:55], v[156:159]\n"
    "v_cvt_pk_bf16_f32 v68, v68, v69\n"
    "v_cvt_pk_bf16_f32 v69, v70, v71\n"
    "s_waitcnt lgkmcnt(6)\n"
    "v_mfma_f32_16x16x32_bf16 v[32:35], v[212:215], v[56:59], v[32:35]\n"
    "v_cvt_pk_bf16_f32 v136, v136, v137\n"
    "v_cvt_pk_bf16_f32 v137, v138, v139\n"
    "v_mfma_f32_16x16x32_bf16 v[96:99], v[24:27], v[56:59], v[96:99]\n"
    "v_cvt_pk_bf16_f32 v138, v196, v197\n"
    "v_cvt_pk_bf16_f32 v139, v198, v199\n"
    "v_mfma_f32_16x16x32_bf16 v[100:103], v[164:167], v[56:59], v[100:103]\n"
    "v_cvt_pk_bf16_f32 v140, v140, v141\n"
    "v_cvt_pk_bf16_f32 v141, v142, v143\n"
    "s_waitcnt lgkmcnt(0)\n"
    "s_waitcnt vmcnt(0)\n"
    "s_barrier\n"
    "v_mfma_f32_16x16x32_bf16 v[232:235], v[164:167], v[208:211], v[232:235]\n"
    "ds_read_b64_tr_b16 v[12:13], v248 offset:32768\n"
    "ds_read_b64_tr_b16 v[14:15], v248 offset:36352\n"
    "v_mfma_f32_16x16x32_bf16 v[60:63], v[24:27], v[208:211], v[60:63]\n"
    "ds_read_b128 v[104:107], v252 offset:12288\n"
    "ds_read_b128 v[188:191], v252 offset:14336\n"
    "v_mfma_f32_16x16x32_bf16 v[84:87], v[212:215], v[208:211], v[84:87]\n"
    "ds_read_b128 v[108:111], v252 offset:16384\n"
    "ds_read_b64_tr_b16 v[112:113], v248 offset:32800\n"
    "v_mfma_f32_16x16x32_bf16 v[184:187], v[212:215], v[64:67], v[184:187]\n"
    "ds_read_b64_tr_b16 v[114:115], v248 offset:36384\n"
    "ds_read_b64_tr_b16 v[236:237], v248 offset:32832\n"
    "v_mfma_f32_16x16x32_bf16 v[200:203], v[24:27], v[64:67], v[200:203]\n"
    "ds_read_b64_tr_b16 v[238:239], v248 offset:36416\n"
    "v_cvt_pk_bf16_f32 v156, v156, v157\n"
    "v_cvt_pk_bf16_f32 v157, v158, v159\n"
    "v_mfma_f32_16x16x32_bf16 v[244:247], v[164:167], v[64:67], v[244:247]\n"
    "v_cvt_pk_bf16_f32 v158, v160, v161\n"
    "v_cvt_pk_bf16_f32 v159, v162, v163\n"
    "v_mfma_f32_16x16x32_bf16 v[128:131], v[164:167], v[216:219], v[128:131]\n"
    "ds_read_b64_tr_b16 v[88:89], v248 offset:32864\n"
    "ds_read_b64_tr_b16 v[90:91], v248 offset:36448\n"
    "v_mfma_f32_16x16x32_bf16 v[124:127], v[24:27], v[216:219], v[124:127]\n"
    "v_cvt_pk_bf16_f32 v28, v28, v29\n"
    "v_cvt_pk_bf16_f32 v29, v30, v31\n"
    "v_mfma_f32_16x16x32_bf16 v[120:123], v[212:215], v[216:219], v[120:123]\n"
    "v_cvt_pk_bf16_f32 v32, v32, v33\n"
    "v_cvt_pk_bf16_f32 v33, v34, v35\n"
    "s_waitcnt lgkmcnt(8)\n"
    "v_mfma_f32_16x16x32_bf16 v[180:183], v[104:107], v[12:15], 0\n"
    "ds_read_b64_tr_b16 v[0:1], v248 offset:32896\n"
    "ds_read_b64_tr_b16 v[2:3], v248 offset:36480\n"
    "s_waitcnt lgkmcnt(9)\n"
    "v_mfma_f32_16x16x32_bf16 v[228:231], v[188:191], v[12:15], 0\n"
    "v_cvt_pk_bf16_f32 v34, v96, v97\n"
    "v_cvt_pk_bf16_f32 v35, v98, v99\n"
    "s_waitcnt lgkmcnt(8)\n"
    "v_mfma_f32_16x16x32_bf16 v[92:95], v[108:111], v[12:15], 0\n"
    "v_cvt_pk_bf16_f32 v100, v100, v101\n"
    "v_cvt_pk_bf16_f32 v101, v102, v103\n"
    "s_waitcnt lgkmcnt(6)\n"
    "v_mfma_f32_16x16x32_bf16 v[176:179], v[108:111], v[112:115], 0\n"
    "ds_read_b64_tr_b16 v[240:241], v248 offset:32928\n"
    "ds_read_b64_tr_b16 v[242:243], v248 offset:36512\n"
    "v_mfma_f32_16x16x32_bf16 v[116:119], v[188:191], v[112:115], 0\n"
    "v_cvt_pk_bf16_f32 v84, v84, v85\n"
    "v_cvt_pk_bf16_f32 v85, v86, v87\n"
    "v_mfma_f32_16x16x32_bf16 v[144:147], v[104:107], v[112:115], 0\n"
    "v_cvt_pk_bf16_f32 v86, v60, v61\n"
    "v_cvt_pk_bf16_f32 v87, v62, v63\n"
    "s_waitcnt lgkmcnt(6)\n"
    "v_mfma_f32_16x16x32_bf16 v[148:151], v[104:107], v[236:239], 0\n"
    "ds_read_b64_tr_b16 v[8:9], v248 offset:32960\n"
    "ds_read_b64_tr_b16 v[10:11], v248 offset:36544\n"
    "v_mfma_f32_16x16x32_bf16 v[152:155], v[188:191], v[236:239], 0\n"
    "v_cvt_pk_bf16_f32 v232, v232, v233\n"
    "v_cvt_pk_bf16_f32 v233, v234, v235\n"
    "v_mfma_f32_16x16x32_bf16 v[4:7], v[108:111], v[236:239], 0\n"
    "v_cvt_pk_bf16_f32 v184, v184, v185\n"
    "v_cvt_pk_bf16_f32 v185, v186, v187\n"
    "s_waitcnt lgkmcnt(6)\n"
    "v_mfma_f32_16x16x32_bf16 v[48:51], v[108:111], v[88:91], 0\n"
    "ds_read_b64_tr_b16 v[204:205], v248 offset:39936\n"
    "ds_read_b64_tr_b16 v[206:207], v248 offset:43520\n"
    "v_mfma_f32_16x16x32_bf16 v[172:175], v[188:191], v[88:91], 0\n"
    "v_cvt_pk_bf16_f32 v186, v200, v201\n"
    "v_cvt_pk_bf16_f32 v187, v202, v203\n"
    "v_mfma_f32_16x16x32_bf16 v[168:171], v[104:107], v[88:91], 0\n"
    "v_cvt_pk_bf16_f32 v244, v244, v245\n"
    "v_cvt_pk_bf16_f32 v245, v246, v247\n"
    "s_waitcnt lgkmcnt(6)\n"
    "v_mfma_f32_16x16x32_bf16 v[20:23], v[104:107], v[0:3], 0\n"
    "ds_read_b64_tr_b16 v[40:41], v248 offset:39968\n"
    "ds_read_b64_tr_b16 v[42:43], v248 offset:43552\n"
    "v_mfma_f32_16x16x32_bf16 v[36:39], v[188:191], v[0:3], 0\n"
    "ds_read_b128 v[44:47], v253 offset:12288\n"
    "ds_read_b128 v[220:223], v253 offset:14336\n"
    "v_mfma_f32_16x16x32_bf16 v[192:195], v[108:111], v[0:3], 0\n"
    "ds_read_b128 v[16:19], v253 offset:16384\n"
    "v_cvt_pk_bf16_f32 v120, v120, v121\n"
    "v_cvt_pk_bf16_f32 v121, v122, v123\n"
    "s_waitcnt lgkmcnt(9)\n"
    "v_mfma_f32_16x16x32_bf16 v[76:79], v[108:111], v[240:243], 0\n"
    "ds_read_b64_tr_b16 v[132:133], v248 offset:40000\n"
    "ds_read_b64_tr_b16 v[134:135], v248 offset:43584\n"
    "v_mfma_f32_16x16x32_bf16 v[72:75], v[188:191], v[240:243], 0\n"
    "v_cvt_pk_bf16_f32 v122, v124, v125\n"
    "v_cvt_pk_bf16_f32 v123, v126, v127\n"
    "v_mfma_f32_16x16x32_bf16 v[224:227], v[104:107], v[240:243], 0\n"
    "v_cvt_pk_bf16_f32 v128, v128, v129\n"
    "v_cvt_pk_bf16_f32 v129, v130, v131\n"
    "s_waitcnt lgkmcnt(9)\n"
    "v_mfma_f32_16x16x32_bf16 v[52:55], v[104:107], v[8:11], 0\n"
    "ds_read_b64_tr_b16 v[208:209], v248 offset:40032\n"
    "ds_read_b64_tr_b16 v[210:211], v248 offset:43616\n"
    "v_mfma_f32_16x16x32_bf16 v[196:199], v[188:191], v[8:11], 0\n"
    "v_mfma_f32_16x16x32_bf16 v[56:59], v[108:111], v[8:11], 0\n"
    "s_waitcnt lgkmcnt(4)\n"
    "v_mfma_f32_16x16x32_bf16 v[92:95], v[16:19], v[204:207], v[92:95]\n"
    "ds_read_b64_tr_b16 v[160:161], v248 offset:40064\n"
    "ds_read_b64_tr_b16 v[162:163], v248 offset:43648\n"
    "v_mfma_f32_16x16x32_bf16 v[228:231], v[220:223], v[204:207], v[228:231]\n"
    "v_mfma_f32_16x16x32_bf16 v[180:183], v[44:47], v[204:207], v[180:183]\n"
    "v_mfma_f32_16x16x32_bf16 v[144:147], v[44:47], v[40:43], v[144:147]\n"
    "ds_read_b64_tr_b16 v[64:65], v248 offset:40096\n"
    "ds_read_b64_tr_b16 v[66:67], v248 offset:43680\n"
    "v_mfma_f32_16x16x32_bf16 v[116:119], v[220:223], v[40:43], v[116:119]\n"
    "v_mfma_f32_16x16x32_bf16 v[176:179], v[16:19], v[40:43], v[176:179]\n"
    "s_waitcnt lgkmcnt(6)\n"
    "v_mfma_f32_16x16x32_bf16 v[4:7], v[16:19], v[132:135], v[4:7]\n"
    "ds_read_b64_tr_b16 v[216:217], v248 offset:40128\n"
    "ds_read_b64_tr_b16 v[218:219], v248 offset:43712\n"
    "v_mfma_f32_16x16x32_bf16 v[152:155], v[220:223], v[132:135], v[152:155]\n"
    "v_mfma_f32_16x16x32_bf16 v[148:151], v[44:47], v[132:135], v[148:151]\n"
    "s_waitcnt lgkmcnt(6)\n"
    "v_mfma_f32_16x16x32_bf16 v[168:171], v[44:47], v[208:211], v[168:171]\n"
    "v_mfma_f32_16x16x32_bf16 v[172:175], v[220:223], v[208:211], v[172:175]\n"
    "v_mfma_f32_16x16x32_bf16 v[48:51], v[16:19], v[208:211], v[48:51]\n"
    "s_waitcnt lgkmcnt(0)\n"
    "s_barrier\n"
    "v_mfma_f32_16x16x32_bf16 v[192:195], v[16:19], v[160:163], v[192:195]\n"
    "ds_read_b64_tr_b16 v[212:213], v248 offset:49152\n"
    "ds_read_b64_tr_b16 v[214:215], v248 offset:52736\n"
    "v_mfma_f32_16x16x32_bf16 v[36:39], v[220:223], v[160:163], v[36:39]\n"
    "ds_read_b128 v[24:27], v252 offset:18432\n"
    "ds_read_b128 v[164:167], v252 offset:20480\n"
    "v_mfma_f32_16x16x32_bf16 v[20:23], v[44:47], v[160:163], v[20:23]\n"
    "ds_read_b128 v[96:99], v252 offset:22528\n"
    "ds_read_b64_tr_b16 v[12:13], v248 offset:49184\n"
    "v_mfma_f32_16x16x32_bf16 v[224:227], v[44:47], v[64:67], v[224:227]\n"
    "ds_read_b64_tr_b16 v[14:15], v248 offset:52768\n"
    "ds_read_b64_tr_b16 v[60:61], v248 offset:49216\n"
    "v_mfma_f32_16x16x32_bf16 v[72:75], v[220:223], v[64:67], v[72:75]\n"
    "ds_read_b64_tr_b16 v[62:63], v248 offset:52800\n"
    "v_mfma_f32_16x16x32_bf16 v[76:79], v[16:19], v[64:67], v[76:79]\n"
    "v_mfma_f32_16x16x32_bf16 v[56:59], v[16:19], v[216:219], v[56:59]\n"
    "ds_read_b64_tr_b16 v[112:113], v248 offset:49248\n"
    "ds_read_b64_tr_b16 v[114:115], v248 offset:52832\n"
    "v_mfma_f32_16x16x32_bf16 v[196:199], v[220:223], v[216:219], v[196:199]\n"
    "v_mfma_f32_16x16x32_bf16 v[52:55], v[44:47], v[216:219], v[52:55]\n"
    "s_waitcnt lgkmcnt(8)\n"
    "v_mfma_f32_16x16x32_bf16 v[180:183], v[24:27], v[212:215], v[180:183]\n"
    "ds_read_b64_tr_b16 v[236:237], v248 offset:49280\n"
    "ds_read_b64_tr_b16 v[238:239], v248 offset:52864\n"
    "s_waitcnt lgkmcnt(9)\n"
    "v_mfma_f32_16x16x32_bf16 v[228:231], v[164:167], v[212:215], v[228:231]\n"
    "s_waitcnt lgkmcnt(8)\n"
    "v_mfma_f32_16x16x32_bf16 v[92:95], v[96:99], v[212:215], v[92:95]\n"
    "s_waitcnt lgkmcnt(6)\n"
    "v_mfma_f32_16x16x32_bf16 v[176:179], v[96:99], v[12:15], v[176:179]\n"
    "ds_read_b64_tr_b16 v[200:201], v248 offset:49312\n"
    "ds_read_b64_tr_b16 v[202:203], v248 offset:52896\n"
    "v_mfma_f32_16x16x32_bf16 v[116:119], v[164:167], v[12:15], v[116:119]\n"
    "v_mfma_f32_16x16x32_bf16 v[144:147], v[24:27], v[12:15], v[144:147]\n"
    "s_waitcnt lgkmcnt(6)\n"
    "v_mfma_f32_16x16x32_bf16 v[148:151], v[24:27], v[60:63], v[148:151]\n"
    "ds_read_b64_tr_b16 v[88:89], v248 offset:49344\n"
    "ds_read_b64_tr_b16 v[90:91], v248 offset:52928\n"
    "v_mfma_f32_16x16x32_bf16 v[152:155], v[164:167], v[60:63], v[152:155]\n"
    "v_mfma_f32_16x16x32_bf16 v[4:7], v[96:99], v[60:63], v[4:7]\n"
    "s_waitcnt lgkmcnt(6)\n"
    "v_mfma_f32_16x16x32_bf16 v[48:51], v[96:99], v[112:115], v[48:51]\n"
    "ds_read_b64_tr_b16 v[0:1], v248 offset:56320\n"
    "ds_read_b64_tr_b16 v[2:3], v248 offset:59904\n"
    "v_mfma_f32_16x16x32_bf16 v[172:175], v[164:167], v[112:115], v[172:175]\n"
    "v_mfma_f32_16x16x32_bf16 v[168:171], v[24:27], v[112:115], v[168:171]\n"
    "s_waitcnt lgkmcnt(6)\n"
    "v_mfma_f32_16x16x32_bf16 v[20:23], v[24:27], v[236:239], v[20:23]\n"
    "ds_read_b64_tr_b16 v[124:125], v248 offset:56352\n"
    "ds_read_b64_tr_b16 v[126:127], v248 offset:59936\n"
    "v_mfma_f32_16x16x32_bf16 v[36:39], v[164:167], v[236:239], v[36:39]\n"
    "ds_read_b128 v[240:243], v253 offset:18432\n"
    "ds_read_b128 v[8:11], v253 offset:20480\n"
    "v_mfma_f32_16x16x32_bf16 v[192:195], v[96:99], v[236:239], v[192:195]\n"
    "ds_read_b128 v[104:107], v253 offset:22528\n"
    "s_waitcnt lgkmcnt(9)\n"
    "v_mfma_f32_16x16x32_bf16 v[76:79], v[96:99], v[200:203], v[76:79]\n"
    "ds_read_b64_tr_b16 v[188:189], v248 offset:56384\n"
    "ds_read_b64_tr_b16 v[190:191], v248 offset:59968\n"
    "v_mfma_f32_16x16x32_bf16 v[72:75], v[164:167], v[200:203], v[72:75]\n"
    "v_mfma_f32_16x16x32_bf16 v[224:227], v[24:27], v[200:203], v[224:227]\n"
    "s_waitcnt lgkmcnt(9)\n"
    "v_mfma_f32_16x16x32_bf16 v[52:55], v[24:27], v[88:91], v[52:55]\n"
    "ds_read_b64_tr_b16 v[108:109], v248 offset:56416\n"
    "ds_read_b64_tr_b16 v[110:111], v248 offset:60000\n"
    "v_mfma_f32_16x16x32_bf16 v[196:199], v[164:167], v[88:91], v[196:199]\n"
    "v_mfma_f32_16x16x32_bf16 v[56:59], v[96:99], v[88:91], v[56:59]\n"
    "s_waitcnt lgkmcnt(4)\n"
    "v_mfma_f32_16x16x32_bf16 v[92:95], v[104:107], v[0:3], v[92:95]\n"
    "ds_read_b64_tr_b16 v[204:205], v248 offset:56448\n"
    "ds_read_b64_tr_b16 v[206:207], v248 offset:60032\n"
    "v_mfma_f32_16x16x32_bf16 v[228:231], v[8:11], v[0:3], v[228:231]\n"
    "v_mfma_f32_16x16x32_bf16 v[180:183], v[240:243], v[0:3], v[180:183]\n"
    "v_mfma_f32_16x16x32_bf16 v[144:147], v[240:243], v[124:127], v[144:147]\n"
    "ds_read_b64_tr_b16 v[40:41], v248 offset:56480\n"
    "ds_read_b64_tr_b16 v[42:43], v248 offset:60064\n"
    "v_mfma_f32_16x16x32_bf16 v[116:119], v[8:11], v[124:127], v[116:119]\n"
    "v_mfma_f32_16x16x32_bf16 v[176:179], v[104:107], v[124:127], v[176:179]\n"
    "s_waitcnt lgkmcnt(6)\n"
    "v_mfma_f32_16x16x32_bf16 v[4:7], v[104:107], v[188:191], v[4:7]\n"
    "ds_read_b64_tr_b16 v[132:133], v248 offset:56512\n"
    "ds_read_b64_tr_b16 v[134:135], v248 offset:60096\n"
    "v_mfma_f32_16x16x32_bf16 v[152:155], v[8:11], v[188:191], v[152:155]\n"
    "v_mfma_f32_16x16x32_bf16 v[148:151], v[240:243], v[188:191], v[148:151]\n"
    "s_waitcnt lgkmcnt(6)\n"
    "v_mfma_f32_16x16x32_bf16 v[168:171], v[240:243], v[108:111], v[168:171]\n"
    "v_mfma_f32_16x16x32_bf16 v[172:175], v[8:11], v[108:111], v[172:175]\n"
    "v_mfma_f32_16x16x32_bf16 v[48:51], v[104:107], v[108:111], v[48:51]\n"
    "s_waitcnt lgkmcnt(0)\n"
    "s_barrier\n"
    "v_mfma_f32_16x16x32_bf16 v[192:195], v[104:107], v[204:207], v[192:195]\n"
    "ds_read_b64_tr_b16 v[208:209], v248 offset:0\n"
    "ds_read_b64_tr_b16 v[210:211], v248 offset:3584\n"
    "v_mfma_f32_16x16x32_bf16 v[36:39], v[8:11], v[204:207], v[36:39]\n"
    "ds_read_b128 v[160:163], v252 offset:6144\n"
    "ds_read_b128 v[64:67], v252 offset:8192\n"
    "v_mfma_f32_16x16x32_bf16 v[20:23], v[240:243], v[204:207], v[20:23]\n"
    "ds_read_b128 v[216:219], v252 offset:10240\n"
    "ds_read_b64_tr_b16 v[44:45], v248 offset:32\n"
    "v_mfma_f32_16x16x32_bf16 v[224:227], v[240:243], v[40:43], v[224:227]\n"
    "ds_read_b64_tr_b16 v[46:47], v248 offset:3616\n"
    "ds_read_b64_tr_b16 v[220:221], v248 offset:64\n"
    "v_mfma_f32_16x16x32_bf16 v[72:75], v[8:11], v[40:43], v[72:75]\n"
    "ds_read_b64_tr_b16 v[222:223], v248 offset:3648\n"
    "v_mfma_f32_16x16x32_bf16 v[76:79], v[104:107], v[40:43], v[76:79]\n"
    "v_mfma_f32_16x16x32_bf16 v[56:59], v[104:107], v[132:135], v[56:59]\n"
    "ds_read_b64_tr_b16 v[16:17], v248 offset:96\n"
    "ds_read_b64_tr_b16 v[18:19], v248 offset:3680\n"
    "v_mfma_f32_16x16x32_bf16 v[196:199], v[8:11], v[132:135], v[196:199]\n"
    "v_mfma_f32_16x16x32_bf16 v[52:55], v[240:243], v[132:135], v[52:55]\n"
    "s_waitcnt lgkmcnt(8)\n"
    "v_mfma_f32_16x16x32_bf16 v[180:183], v[160:163], v[208:211], v[180:183]\n"
    "ds_read_b64_tr_b16 v[212:213], v248 offset:128\n"
    "ds_read_b64_tr_b16 v[214:215], v248 offset:3712\n"
    "s_waitcnt lgkmcnt(9)\n"
    "v_mfma_f32_16x16x32_bf16 v[228:231], v[64:67], v[208:211], v[228:231]\n"
    "s_waitcnt lgkmcnt(8)\n"
    "v_mfma_f32_16x16x32_bf16 v[92:95], v[216:219], v[208:211], v[92:95]\n"
    "s_waitcnt lgkmcnt(6)\n"
    "v_mfma_f32_16x16x32_bf16 v[176:179], v[216:219], v[44:47], v[176:179]\n"
    "ds_read_b64_tr_b16 v[12:13], v248 offset:160\n"
    "ds_read_b64_tr_b16 v[14:15], v248 offset:3744\n"
    "v_mfma_f32_16x16x32_bf16 v[116:119], v[64:67], v[44:47], v[116:119]\n"
    "v_mfma_f32_16x16x32_bf16 v[144:147], v[160:163], v[44:47], v[144:147]\n"
    "s_waitcnt lgkmcnt(6)\n"
    "v_mfma_f32_16x16x32_bf16 v[148:151], v[160:163], v[220:223], v[148:151]\n"
    "ds_read_b64_tr_b16 v[60:61], v248 offset:192\n"
    "ds_read_b64_tr_b16 v[62:63], v248 offset:3776\n"
    "v_mfma_f32_16x16x32_bf16 v[152:155], v[64:67], v[220:223], v[152:155]\n"
    "v_mfma_f32_16x16x32_bf16 v[4:7], v[216:219], v[220:223], v[4:7]\n"
    "s_waitcnt lgkmcnt(6)\n"
    "v_mfma_f32_16x16x32_bf16 v[48:51], v[216:219], v[16:19], v[48:51]\n"
    "ds_read_b64_tr_b16 v[112:113], v248 offset:7168\n"
    "ds_read_b64_tr_b16 v[114:115], v248 offset:10752\n"
    "v_mfma_f32_16x16x32_bf16 v[172:175], v[64:67], v[16:19], v[172:175]\n"
    "v_mfma_f32_16x16x32_bf16 v[168:171], v[160:163], v[16:19], v[168:171]\n"
    "s_waitcnt lgkmcnt(6)\n"
    "v_mfma_f32_16x16x32_bf16 v[20:23], v[160:163], v[212:215], v[20:23]\n"
    "ds_read_b64_tr_b16 v[236:237], v248 offset:7200\n"
    "ds_read_b64_tr_b16 v[238:239], v248 offset:10784\n"
    "v_mfma_f32_16x16x32_bf16 v[36:39], v[64:67], v[212:215], v[36:39]\n"
    "ds_read_b128 v[200:203], v253 offset:6144\n"
    "ds_read_b128 v[88:91], v253 offset:8192\n"
    "v_mfma_f32_16x16x32_bf16 v[192:195], v[216:219], v[212:215], v[192:195]\n"
    "ds_read_b128 v[24:27], v253 offset:10240\n"
    "s_waitcnt lgkmcnt(9)\n"
    "v_mfma_f32_16x16x32_bf16 v[76:79], v[216:219], v[12:15], v[76:79]\n"
    "ds_read_b64_tr_b16 v[164:165], v248 offset:7232\n"
    "ds_read_b64_tr_b16 v[166:167], v248 offset:10816\n"
    "v_mfma_f32_16x16x32_bf16 v[72:75], v[64:67], v[12:15], v[72:75]\n"
    "v_mfma_f32_16x16x32_bf16 v[224:227], v[160:163], v[12:15], v[224:227]\n"
    "s_waitcnt lgkmcnt(9)\n"
    "v_mfma_f32_16x16x32_bf16 v[52:55], v[160:163], v[60:63], v[52:55]\n"
    "ds_read_b64_tr_b16 v[96:97], v248 offset:7264\n"
    "ds_read_b64_tr_b16 v[98:99], v248 offset:10848\n"
    "v_mfma_f32_16x16x32_bf16 v[196:199], v[64:67], v[60:63], v[196:199]\n"
    "v_mfma_f32_16x16x32_bf16 v[56:59], v[216:219], v[60:63], v[56:59]\n"
    "s_waitcnt lgkmcnt(4)\n"
    "v_mfma_f32_16x16x32_bf16 v[92:95], v[24:27], v[112:115], v[92:95]\n"
    "ds_read_b64_tr_b16 v[0:1], v248 offset:7296\n"
    "ds_read_b64_tr_b16 v[2:3], v248 offset:10880\n"
    "v_mfma_f32_16x16x32_bf16 v[228:231], v[88:91], v[112:115], v[228:231]\n"
    "v_mfma_f32_16x16x32_bf16 v[180:183], v[200:203], v[112:115], v[180:183]\n"
    "v_mfma_f32_16x16x32_bf16 v[144:147], v[200:203], v[236:239], v[144:147]\n"
    "ds_read_b64_tr_b16 v[124:125], v248 offset:7328\n"
    "ds_read_b64_tr_b16 v[126:127], v248 offset:10912\n"
    "v_mfma_f32_16x16x32_bf16 v[116:119], v[88:91], v[236:239], v[116:119]\n"
    "v_mfma_f32_16x16x32_bf16 v[176:179], v[24:27], v[236:239], v[176:179]\n"
    "s_waitcnt lgkmcnt(6)\n"
    "v_mfma_f32_16x16x32_bf16 v[4:7], v[24:27], v[164:167], v[4:7]\n"
    "ds_read_b64_tr_b16 v[188:189], v248 offset:7360\n"
    "ds_read_b64_tr_b16 v[190:191], v248 offset:10944\n"
    "v_mfma_f32_16x16x32_bf16 v[152:155], v[88:91], v[164:167], v[152:155]\n"
    "v_mfma_f32_16x16x32_bf16 v[148:151], v[200:203], v[164:167], v[148:151]\n"
    "s_waitcnt lgkmcnt(6)\n"
    "v_mfma_f32_16x16x32_bf16 v[168:171], v[200:203], v[96:99], v[168:171]\n"
    "v_mfma_f32_16x16x32_bf16 v[172:175], v[88:91], v[96:99], v[172:175]\n"
    "v_mfma_f32_16x16x32_bf16 v[48:51], v[24:27], v[96:99], v[48:51]\n"
    "s_waitcnt lgkmcnt(0)\n"
    "s_barrier\n"
    "v_mfma_f32_16x16x32_bf16 v[192:195], v[24:27], v[0:3], v[192:195]\n"
    "ds_read_b64_tr_b16 v[108:109], v248 offset:16384\n"
    "ds_read_b64_tr_b16 v[110:111], v248 offset:19968\n"
    "v_mfma_f32_16x16x32_bf16 v[36:39], v[88:91], v[0:3], v[36:39]\n"
    "ds_read_b128 v[204:207], v252 offset:0\n"
    "ds_read_b128 v[40:43], v252 offset:2048\n"
    "v_mfma_f32_16x16x32_bf16 v[20:23], v[200:203], v[0:3], v[20:23]\n"
    "ds_read_b128 v[132:135], v252 offset:4096\n"
    "ds_read_b64_tr_b16 v[240:241], v248 offset:16416\n"
    "v_mfma_f32_16x16x32_bf16 v[224:227], v[200:203], v[124:127], v[224:227]\n"
    "ds_read_b64_tr_b16 v[242:243], v248 offset:20000\n"
    "ds_read_b64_tr_b16 v[8:9], v248 offset:16448\n"
    "v_mfma_f32_16x16x32_bf16 v[72:75], v[88:91], v[124:127], v[72:75]\n"
    "ds_read_b64_tr_b16 v[10:11], v248 offset:20032\n"
    "v_mfma_f32_16x16x32_bf16 v[76:79], v[24:27], v[124:127], v[76:79]\n"
    "v_mfma_f32_16x16x32_bf16 v[56:59], v[24:27], v[188:191], v[56:59]\n"
    "ds_read_b64_tr_b16 v[104:105], v248 offset:16480\n"
    "ds_read_b64_tr_b16 v[106:107], v248 offset:20064\n"
    "v_mfma_f32_16x16x32_bf16 v[196:199], v[88:91], v[188:191], v[196:199]\n"
    "v_mfma_f32_16x16x32_bf16 v[52:55], v[200:203], v[188:191], v[52:55]\n"
    "s_waitcnt lgkmcnt(8)\n"
    "v_mfma_f32_16x16x32_bf16 v[180:183], v[204:207], v[108:111], v[180:183]\n"
    "ds_read_b64_tr_b16 v[208:209], v248 offset:16512\n"
    "ds_read_b64_tr_b16 v[210:211], v248 offset:20096\n"
    "s_waitcnt lgkmcnt(9)\n"
    "v_mfma_f32_16x16x32_bf16 v[228:231], v[40:43], v[108:111], v[228:231]\n"
    "s_waitcnt lgkmcnt(8)\n"
    "v_mfma_f32_16x16x32_bf16 v[92:95], v[132:135], v[108:111], v[92:95]\n"
    "s_waitcnt lgkmcnt(6)\n"
    "v_mfma_f32_16x16x32_bf16 v[176:179], v[132:135], v[240:243], v[176:179]\n"
    "ds_read_b64_tr_b16 v[44:45], v248 offset:16544\n"
    "ds_read_b64_tr_b16 v[46:47], v248 offset:20128\n"
    "v_mfma_f32_16x16x32_bf16 v[116:119], v[40:43], v[240:243], v[116:119]\n"
    "v_mfma_f32_16x16x32_bf16 v[144:147], v[204:207], v[240:243], v[144:147]\n"
    "s_waitcnt lgkmcnt(6)\n"
    "v_mfma_f32_16x16x32_bf16 v[148:151], v[204:207], v[8:11], v[148:151]\n"
    "ds_read_b64_tr_b16 v[220:221], v248 offset:16576\n"
    "ds_read_b64_tr_b16 v[222:223], v248 offset:20160\n"
    "v_mfma_f32_16x16x32_bf16 v[152:155], v[40:43], v[8:11], v[152:155]\n"
    "v_mfma_f32_16x16x32_bf16 v[4:7], v[132:135], v[8:11], v[4:7]\n"
    "s_waitcnt lgkmcnt(6)\n"
    "v_mfma_f32_16x16x32_bf16 v[48:51], v[132:135], v[104:107], v[48:51]\n"
    "ds_read_b64_tr_b16 v[16:17], v248 offset:23552\n"
    "ds_read_b64_tr_b16 v[18:19], v248 offset:27136\n"
    "v_mfma_f32_16x16x32_bf16 v[172:175], v[40:43], v[104:107], v[172:175]\n"
    "v_mfma_f32_16x16x32_bf16 v[168:171], v[204:207], v[104:107], v[168:171]\n"
    "s_waitcnt lgkmcnt(6)\n"
    "v_mfma_f32_16x16x32_bf16 v[20:23], v[204:207], v[208:211], v[20:23]\n"
    "ds_read_b64_tr_b16 v[212:213], v248 offset:23584\n"
    "ds_read_b64_tr_b16 v[214:215], v248 offset:27168\n"
    "v_mfma_f32_16x16x32_bf16 v[36:39], v[40:43], v[208:211], v[36:39]\n"
    "ds_read_b128 v[12:15], v253 offset:0\n"
    "ds_read_b128 v[60:63], v253 offset:2048\n"
    "v_mfma_f32_16x16x32_bf16 v[192:195], v[132:135], v[208:211], v[192:195]\n"
    "ds_read_b128 v[160:163], v253 offset:4096\n"
    "s_waitcnt lgkmcnt(9)\n"
    "v_mfma_f32_16x16x32_bf16 v[76:79], v[132:135], v[44:47], v[76:79]\n"
    "ds_read_b64_tr_b16 v[64:65], v248 offset:23616\n"
    "ds_read_b64_tr_b16 v[66:67], v248 offset:27200\n"
    "v_mfma_f32_16x16x32_bf16 v[72:75], v[40:43], v[44:47], v[72:75]\n"
    "v_mfma_f32_16x16x32_bf16 v[224:227], v[204:207], v[44:47], v[224:227]\n"
    "s_waitcnt lgkmcnt(9)\n"
    "v_mfma_f32_16x16x32_bf16 v[52:55], v[204:207], v[220:223], v[52:55]\n"
    "ds_read_b64_tr_b16 v[216:217], v248 offset:23648\n"
    "ds_read_b64_tr_b16 v[218:219], v248 offset:27232\n"
    "v_mfma_f32_16x16x32_bf16 v[196:199], v[40:43], v[220:223], v[196:199]\n"
    "v_mfma_f32_16x16x32_bf16 v[56:59], v[132:135], v[220:223], v[56:59]\n"
    "s_waitcnt lgkmcnt(4)\n"
    "v_mfma_f32_16x16x32_bf16 v[92:95], v[160:163], v[16:19], v[92:95]\n"
    "ds_read_b64_tr_b16 v[112:113], v248 offset:23680\n"
    "ds_read_b64_tr_b16 v[114:115], v248 offset:27264\n"
    "v_mfma_f32_16x16x32_bf16 v[228:231], v[60:63], v[16:19], v[228:231]\n"
    "v_mfma_f32_16x16x32_bf16 v[180:183], v[12:15], v[16:19], v[180:183]\n"
    "v_mfma_f32_16x16x32_bf16 v[144:147], v[12:15], v[212:215], v[144:147]\n"
    "ds_read_b64_tr_b16 v[236:237], v248 offset:23712\n"
    "ds_read_b64_tr_b16 v[238:239], v248 offset:27296\n"
    "v_mfma_f32_16x16x32_bf16 v[116:119], v[60:63], v[212:215], v[116:119]\n"
    "v_mfma_f32_16x16x32_bf16 v[176:179], v[160:163], v[212:215], v[176:179]\n"
    "s_nop 2\n"
    "v_cvt_pk_bf16_f32 v180, v180, v181\n"
    "v_cvt_pk_bf16_f32 v181, v182, v183\n"
    "s_waitcnt lgkmcnt(6)\n"
    "v_mfma_f32_16x16x32_bf16 v[4:7], v[160:163], v[64:67], v[4:7]\n"
    "ds_read_b64_tr_b16 v[164:165], v248 offset:23744\n"
    "ds_read_b64_tr_b16 v[166:167], v248 offset:27328\n"
    "v_mfma_f32_16x16x32_bf16 v[152:155], v[60:63], v[64:67], v[152:155]\n"
    "v_cvt_pk_bf16_f32 v182, v228, v229\n"
    "v_cvt_pk_bf16_f32 v183, v230, v231\n"
    "v_mfma_f32_16x16x32_bf16 v[148:151], v[12:15], v[64:67], v[148:151]\n"
    "v_cvt_pk_bf16_f32 v70, v92, v93\n"
    "v_cvt_pk_bf16_f32 v71, v94, v95\n"
    "s_waitcnt lgkmcnt(6)\n"
    "v_mfma_f32_16x16x32_bf16 v[168:171], v[12:15], v[216:219], v[168:171]\n"
    "v_cvt_pk_bf16_f32 v144, v144, v145\n"
    "v_cvt_pk_bf16_f32 v145, v146, v147\n"
    "v_mfma_f32_16x16x32_bf16 v[172:175], v[60:63], v[216:219], v[172:175]\n"
    "v_cvt_pk_bf16_f32 v146, v116, v117\n"
    "v_cvt_pk_bf16_f32 v147, v118, v119\n"
    "v_mfma_f32_16x16x32_bf16 v[48:51], v[160:163], v[216:219], v[48:51]\n"
    "v_cvt_pk_bf16_f32 v142, v176, v177\n"
    "v_cvt_pk_bf16_f32 v143, v178, v179\n"
    "s_waitcnt lgkmcnt(0)\n"
    "s_barrier\n"
    "v_mfma_f32_16x16x32_bf16 v[192:195], v[160:163], v[112:115], v[192:195]\n"
    "ds_read_b64_tr_b16 v[96:97], v248 offset:32768\n"
    "ds_read_b64_tr_b16 v[98:99], v248 offset:36352\n"
    "v_mfma_f32_16x16x32_bf16 v[36:39], v[60:63], v[112:115], v[36:39]\n"
    "ds_read_b128 v[0:3], v252 offset:12288\n"
    "ds_read_b128 v[124:127], v252 offset:14336\n"
    "v_mfma_f32_16x16x32_bf16 v[20:23], v[12:15], v[112:115], v[20:23]\n"
    "ds_read_b128 v[188:191], v252 offset:16384\n"
    "ds_read_b64_tr_b16 v[200:201], v248 offset:32800\n"
    "v_mfma_f32_16x16x32_bf16 v[224:227], v[12:15], v[236:239], v[224:227]\n"
    "ds_read_b64_tr_b16 v[202:203], v248 offset:36384\n"
    "ds_read_b64_tr_b16 v[88:89], v248 offset:32832\n"
    "v_mfma_f32_16x16x32_bf16 v[72:75], v[60:63], v[236:239], v[72:75]\n"
    "ds_read_b64_tr_b16 v[90:91], v248 offset:36416\n"
    "v_cvt_pk_bf16_f32 v148, v148, v149\n"
    "v_cvt_pk_bf16_f32 v149, v150, v151\n"
    "v_mfma_f32_16x16x32_bf16 v[76:79], v[160:163], v[236:239], v[76:79]\n"
    "v_cvt_pk_bf16_f32 v150, v152, v153\n"
    "v_cvt_pk_bf16_f32 v151, v154, v155\n"
    "v_mfma_f32_16x16x32_bf16 v[56:59], v[160:163], v[164:167], v[56:59]\n"
    "ds_read_b64_tr_b16 v[24:25], v248 offset:32864\n"
    "ds_read_b64_tr_b16 v[26:27], v248 offset:36448\n"
    "v_mfma_f32_16x16x32_bf16 v[196:199], v[60:63], v[164:167], v[196:199]\n"
    "v_cvt_pk_bf16_f32 v30, v4, v5\n"
    "v_cvt_pk_bf16_f32 v31, v6, v7\n"
    "v_mfma_f32_16x16x32_bf16 v[52:55], v[12:15], v[164:167], v[52:55]\n"
    "v_cvt_pk_bf16_f32 v168, v168, v169\n"
    "v_cvt_pk_bf16_f32 v169, v170, v171\n"
    "s_waitcnt lgkmcnt(8)\n"
    "v_mfma_f32_16x16x32_bf16 v[108:111], v[0:3], v[96:99], 0\n"
    "ds_read_b64_tr_b16 v[104:105], v248 offset:32896\n"
    "ds_read_b64_tr_b16 v[106:107], v248 offset:36480\n"
    "s_waitcnt lgkmcnt(9)\n"
    "v_mfma_f32_16x16x32_bf16 v[240:243], v[124:127], v[96:99], 0\n"
    "v_cvt_pk_bf16_f32 v170, v172, v173\n"
    "v_cvt_pk_bf16_f32 v171, v174, v175\n"
    "s_waitcnt lgkmcnt(8)\n"
    "v_mfma_f32_16x16x32_bf16 v[8:11], v[188:191], v[96:99], 0\n"
    "v_cvt_pk_bf16_f32 v102, v48, v49\n"
    "v_cvt_pk_bf16_f32 v103, v50, v51\n"
    "s_waitcnt lgkmcnt(6)\n"
    "v_mfma_f32_16x16x32_bf16 v[220:223], v[188:191], v[200:203], 0\n"
    "ds_read_b64_tr_b16 v[204:205], v248 offset:32928\n"
    "ds_read_b64_tr_b16 v[206:207], v248 offset:36512\n"
    "v_mfma_f32_16x16x32_bf16 v[44:47], v[124:127], v[200:203], 0\n"
    "v_cvt_pk_bf16_f32 v20, v20, v21\n"
    "v_cvt_pk_bf16_f32 v21, v22, v23\n"
    "v_mfma_f32_16x16x32_bf16 v[208:211], v[0:3], v[200:203], 0\n"
    "v_cvt_pk_bf16_f32 v22, v36, v37\n"
    "v_cvt_pk_bf16_f32 v23, v38, v39\n"
    "s_waitcnt lgkmcnt(6)\n"
    "v_mfma_f32_16x16x32_bf16 v[40:43], v[0:3], v[88:91], 0\n"
    "ds_read_b64_tr_b16 v[212:213], v248 offset:32960\n"
    "ds_read_b64_tr_b16 v[214:215], v248 offset:36544\n"
    "v_mfma_f32_16x16x32_bf16 v[132:135], v[124:127], v[88:91], 0\n"
    "v_cvt_pk_bf16_f32 v234, v192, v193\n"
    "v_cvt_pk_bf16_f32 v235, v194, v195\n"
    "v_mfma_f32_16x16x32_bf16 v[16:19], v[188:191], v[88:91], 0\n"
    "v_cvt_pk_bf16_f32 v224, v224, v225\n"
    "v_cvt_pk_bf16_f32 v225, v226, v227\n"
    "s_waitcnt lgkmcnt(6)\n"
    "v_mfma_f32_16x16x32_bf16 v[64:67], v[188:191], v[24:27], 0\n"
    "ds_read_b64_tr_b16 v[116:117], v248 offset:39936\n"
    "ds_read_b64_tr_b16 v[118:119], v248 offset:43520\n"
    "v_mfma_f32_16x16x32_bf16 v[92:95], v[124:127], v[24:27], 0\n"
    "v_cvt_pk_bf16_f32 v226, v72, v73\n"
    "v_cvt_pk_bf16_f32 v227, v74, v75\n"
    "v_mfma_f32_16x16x32_bf16 v[228:231], v[0:3], v[24:27], 0\n"
    "v_cvt_pk_bf16_f32 v246, v76, v77\n"
    "v_cvt_pk_bf16_f32 v247, v78, v79\n"
    "s_waitcnt lgkmcnt(6)\n"
    "v_mfma_f32_16x16x32_bf16 v[176:179], v[0:3], v[104:107], 0\n"
    "ds_read_b64_tr_b16 v[152:153], v248 offset:39968\n"
    "ds_read_b64_tr_b16 v[154:155], v248 offset:43552\n"
    "v_mfma_f32_16x16x32_bf16 v[216:219], v[124:127], v[104:107], 0\n"
    "ds_read_b128 v[236:239], v253 offset:12288\n"
    "ds_read_b128 v[4:7], v253 offset:14336\n"
    "v_mfma_f32_16x16x32_bf16 v[112:115], v[188:191], v[104:107], 0\n"
    "ds_read_b128 v[164:167], v253 offset:16384\n"
    "v_cvt_pk_bf16_f32 v52, v52, v53\n"
    "v_cvt_pk_bf16_f32 v53, v54, v55\n"
    "s_waitcnt lgkmcnt(9)\n"
    "v_mfma_f32_16x16x32_bf16 v[160:163], v[188:191], v[204:207], 0\n"
    "ds_read_b64_tr_b16 v[172:173], v248 offset:40000\n"
    "ds_read_b64_tr_b16 v[174:175], v248 offset:43584\n"
    "v_mfma_f32_16x16x32_bf16 v[60:63], v[124:127], v[204:207], 0\n"
    "v_cvt_pk_bf16_f32 v54, v196, v197\n"
    "v_cvt_pk_bf16_f32 v55, v198, v199\n"
    "v_mfma_f32_16x16x32_bf16 v[12:15], v[0:3], v[204:207], 0\n"
    "v_cvt_pk_bf16_f32 v130, v56, v57\n"
    "v_cvt_pk_bf16_f32 v131, v58, v59\n"
    "s_waitcnt lgkmcnt(9)\n"
    "v_mfma_f32_16x16x32_bf16 v[48:51], v[0:3], v[212:215], 0\n"
    "ds_read_b64_tr_b16 v[200:201], v248 offset:40032\n"
    "ds_read_b64_tr_b16 v[202:203], v248 offset:43616\n"
    "v_mfma_f32_16x16x32_bf16 v[96:99], v[124:127], v[212:215], 0\n"
    "v_mfma_f32_16x16x32_bf16 v[36:39], v[188:191], v[212:215], 0\n"
    "s_waitcnt lgkmcnt(4)\n"
    "v_mfma_f32_16x16x32_bf16 v[8:11], v[164:167], v[116:119], v[8:11]\n"
    "ds_read_b64_tr_b16 v[192:193], v248 offset:40064\n"
    "ds_read_b64_tr_b16 v[194:195], v248 offset:43648\n"
    "v_mfma_f32_16x16x32_bf16 v[240:243], v[4:7], v[116:119], v[240:243]\n"
    "v_mfma_f32_16x16x32_bf16 v[108:111], v[236:239], v[116:119], v[108:111]\n"
    "v_mfma_f32_16x16x32_bf16 v[208:211], v[236:239], v[152:155], v[208:211]\n"
    "ds_read_b64_tr_b16 v[88:89], v248 offset:40096\n"
    "ds_read_b64_tr_b16 v[90:91], v248 offset:43680\n"
    "v_mfma_f32_16x16x32_bf16 v[44:47], v[4:7], v[152:155], v[44:47]\n"
    "v_mfma_f32_16x16x32_bf16 v[220:223], v[164:167], v[152:155], v[220:223]\n"
    "s_waitcnt lgkmcnt(6)\n"
    "v_mfma_f32_16x16x32_bf16 v[16:19], v[164:167], v[172:175], v[16:19]\n"
    "ds_read_b64_tr_b16 v[72:73], v248 offset:40128\n"
    "ds_read_b64_tr_b16 v[74:75], v248 offset:43712\n"
    "v_mfma_f32_16x16x32_bf16 v[132:135], v[4:7], v[172:175], v[132:135]\n"
    "v_mfma_f32_16x16x32_bf16 v[40:43], v[236:239], v[172:175], v[40:43]\n"
    "s_waitcnt lgkmcnt(6)\n"
    "v_mfma_f32_16x16x32_bf16 v[228:231], v[236:239], v[200:203], v[228:231]\n"
    "v_mfma_f32_16x16x32_bf16 v[92:95], v[4:7], v[200:203], v[92:95]\n"
    "v_mfma_f32_16x16x32_bf16 v[64:67], v[164:167], v[200:203], v[64:67]\n"
    "s_waitcnt lgkmcnt(0)\n"
    "s_barrier\n"
    "v_mfma_f32_16x16x32_bf16 v[112:115], v[164:167], v[192:195], v[112:115]\n"
    "ds_read_b64_tr_b16 v[76:77], v248 offset:49152\n"
    "ds_read_b64_tr_b16 v[78:79], v248 offset:52736\n"
    "v_mfma_f32_16x16x32_bf16 v[216:219], v[4:7], v[192:195], v[216:219]\n"
    "ds_read_b128 v[24:27], v252 offset:18432\n"
    "ds_read_b128 v[104:107], v252 offset:20480\n"
    "v_mfma_f32_16x16x32_bf16 v[176:179], v[236:239], v[192:195], v[176:179]\n"
    "ds_read_b128 v[196:199], v252 offset:22528\n"
    "ds_read_b64_tr_b16 v[56:57], v248 offset:49184\n"
    "v_mfma_f32_16x16x32_bf16 v[12:15], v[236:239], v[88:91], v[12:15]\n"
    "ds_read_b64_tr_b16 v[58:59], v248 offset:52768\n"
    "ds_read_b64_tr_b16 v[204:205], v248 offset:49216\n"
    "v_mfma_f32_16x16x32_bf16 v[60:63], v[4:7], v[88:91], v[60:63]\n"
    "ds_read_b64_tr_b16 v[206:207], v248 offset:52800\n"
    "v_mfma_f32_16x16x32_bf16 v[160:163], v[164:167], v[88:91], v[160:163]\n"
    "v_mfma_f32_16x16x32_bf16 v[36:39], v[164:167], v[72:75], v[36:39]\n"
    "ds_read_b64_tr_b16 v[212:213], v248 offset:49248\n"
    "ds_read_b64_tr_b16 v[214:215], v248 offset:52832\n"
    "v_mfma_f32_16x16x32_bf16 v[96:99], v[4:7], v[72:75], v[96:99]\n"
    "v_mfma_f32_16x16x32_bf16 v[48:51], v[236:239], v[72:75], v[48:51]\n"
    "s_waitcnt lgkmcnt(8)\n"
    "v_mfma_f32_16x16x32_bf16 v[108:111], v[24:27], v[76:79], v[108:111]\n"
    "ds_read_b64_tr_b16 v[0:1], v248 offset:49280\n"
    "ds_read_b64_tr_b16 v[2:3], v248 offset:52864\n"
    "s_waitcnt lgkmcnt(9)\n"
    "v_mfma_f32_16x16x32_bf16 v[240:243], v[104:107], v[76:79], v[240:243]\n"
    "s_waitcnt lgkmcnt(8)\n"
    "v_mfma_f32_16x16x32_bf16 v[8:11], v[196:199], v[76:79], v[8:11]\n"
    "s_waitcnt lgkmcnt(6)\n"
    "v_mfma_f32_16x16x32_bf16 v[220:223], v[196:199], v[56:59], v[220:223]\n"
    "ds_read_b64_tr_b16 v[124:125], v248 offset:49312\n"
    "ds_read_b64_tr_b16 v[126:127], v248 offset:52896\n"
    "v_mfma_f32_16x16x32_bf16 v[44:47], v[104:107], v[56:59], v[44:47]\n"
    "v_mfma_f32_16x16x32_bf16 v[208:211], v[24:27], v[56:59], v[208:211]\n"
    "s_waitcnt lgkmcnt(6)\n"
    "v_mfma_f32_16x16x32_bf16 v[40:43], v[24:27], v[204:207], v[40:43]\n"
    "ds_read_b64_tr_b16 v[188:189], v248 offset:49344\n"
    "ds_read_b64_tr_b16 v[190:191], v248 offset:52928\n"
    "v_mfma_f32_16x16x32_bf16 v[132:135], v[104:107], v[204:207], v[132:135]\n"
    "v_mfma_f32_16x16x32_bf16 v[16:19], v[196:199], v[204:207], v[16:19]\n"
    "s_waitcnt lgkmcnt(6)\n"
    "v_mfma_f32_16x16x32_bf16 v[64:67], v[196:199], v[212:215], v[64:67]\n"
    "ds_read_b64_tr_b16 v[116:117], v248 offset:56320\n"
    "ds_read_b64_tr_b16 v[118:119], v248 offset:59904\n"
    "v_mfma_f32_16x16x32_bf16 v[92:95], v[104:107], v[212:215], v[92:95]\n"
    "v_mfma_f32_16x16x32_bf16 v[228:231], v[24:27], v[212:215], v[228:231]\n"
    "s_waitcnt lgkmcnt(6)\n"
    "v_mfma_f32_16x16x32_bf16 v[176:179], v[24:27], v[0:3], v[176:179]\n"
    "ds_read_b64_tr_b16 v[152:153], v248 offset:56352\n"
    "ds_read_b64_tr_b16 v[154:155], v248 offset:59936\n"
    "v_mfma_f32_16x16x32_bf16 v[216:219], v[104:107], v[0:3], v[216:219]\n"
    "ds_read_b128 v[172:175], v253 offset:18432\n"
    "ds_read_b128 v[200:203], v253 offset:20480\n"
    "v_mfma_f32_16x16x32_bf16 v[112:115], v[196:199], v[0:3], v[112:115]\n"
    "ds_read_b128 v[192:195], v253 offset:22528\n"
    "s_waitcnt lgkmcnt(9)\n"
    "v_mfma_f32_16x16x32_bf16 v[160:163], v[196:199], v[124:127], v[160:163]\n"
    "ds_read_b64_tr_b16 v[88:89], v248 offset:56384\n"
    "ds_read_b64_tr_b16 v[90:91], v248 offset:59968\n"
    "v_mfma_f32_16x16x32_bf16 v[60:63], v[104:107], v[124:127], v[60:63]\n"
    "v_mfma_f32_16x16x32_bf16 v[12:15], v[24:27], v[124:127], v[12:15]\n"
    "s_waitcnt lgkmcnt(9)\n"
    "v_mfma_f32_16x16x32_bf16 v[48:51], v[24:27], v[188:191], v[48:51]\n"
    "ds_read_b64_tr_b16 v[72:73], v248 offset:56416\n"
    "ds_read_b64_tr_b16 v[74:75], v248 offset:60000\n"
    "v_mfma_f32_16x16x32_bf16 v[96:99], v[104:107], v[188:191], v[96:99]\n"
    "v_mfma_f32_16x16x32_bf16 v[36:39], v[196:199], v[188:191], v[36:39]\n"
    "s_waitcnt lgkmcnt(4)\n"
    "v_mfma_f32_16x16x32_bf16 v[8:11], v[192:195], v[116:119], v[8:11]\n"
    "ds_read_b64_tr_b16 v[236:237], v248 offset:56448\n"
    "ds_read_b64_tr_b16 v[238:239], v248 offset:60032\n"
    "v_mfma_f32_16x16x32_bf16 v[240:243], v[200:203], v[116:119], v[240:243]\n"
    "v_mfma_f32_16x16x32_bf16 v[108:111], v[172:175], v[116:119], v[108:111]\n"
    "v_mfma_f32_16x16x32_bf16 v[208:211], v[172:175], v[152:155], v[208:211]\n"
    "ds_read_b64_tr_b16 v[4:5], v248 offset:56480\n"
    "ds_read_b64_tr_b16 v[6:7], v248 offset:60064\n"
    "v_mfma_f32_16x16x32_bf16 v[44:47], v[200:203], v[152:155], v[44:47]\n"
    "v_mfma_f32_16x16x32_bf16 v[220:223], v[192:195], v[152:155], v[220:223]\n"
    "s_waitcnt lgkmcnt(6)\n"
    "v_mfma_f32_16x16x32_bf16 v[16:19], v[192:195], v[88:91], v[16:19]\n"
    "ds_read_b64_tr_b16 v[164:165], v248 offset:56512\n"
    "ds_read_b64_tr_b16 v[166:167], v248 offset:60096\n"
    "v_mfma_f32_16x16x32_bf16 v[132:135], v[200:203], v[88:91], v[132:135]\n"
    "v_mfma_f32_16x16x32_bf16 v[40:43], v[172:175], v[88:91], v[40:43]\n"
    "s_waitcnt lgkmcnt(6)\n"
    "v_mfma_f32_16x16x32_bf16 v[228:231], v[172:175], v[72:75], v[228:231]\n"
    "v_mfma_f32_16x16x32_bf16 v[92:95], v[200:203], v[72:75], v[92:95]\n"
    "v_mfma_f32_16x16x32_bf16 v[64:67], v[192:195], v[72:75], v[64:67]\n"
    "s_waitcnt lgkmcnt(0)\n"
    "s_barrier\n"
    "v_mfma_f32_16x16x32_bf16 v[112:115], v[192:195], v[236:239], v[112:115]\n"
    "ds_read_b64_tr_b16 v[76:77], v248 offset:0\n"
    "ds_read_b64_tr_b16 v[78:79], v248 offset:3584\n"
    "v_mfma_f32_16x16x32_bf16 v[216:219], v[200:203], v[236:239], v[216:219]\n"
    "ds_read_b128 v[56:59], v252 offset:6144\n"
    "ds_read_b128 v[204:207], v252 offset:8192\n"
    "v_mfma_f32_16x16x32_bf16 v[176:179], v[172:175], v[236:239], v[176:179]\n"
    "ds_read_b128 v[212:215], v252 offset:10240\n"
    "ds_read_b64_tr_b16 v[0:1], v248 offset:32\n"
    "v_mfma_f32_16x16x32_bf16 v[12:15], v[172:175], v[4:7], v[12:15]\n"
    "ds_read_b64_tr_b16 v[2:3], v248 offset:3616\n"
    "ds_read_b64_tr_b16 v[124:125], v248 offset:64\n"
    "v_mfma_f32_16x16x32_bf16 v[60:63], v[200:203], v[4:7], v[60:63]\n"
    "ds_read_b64_tr_b16 v[126:127], v248 offset:3648\n"
    "v_mfma_f32_16x16x32_bf16 v[160:163], v[192:195], v[4:7], v[160:163]\n"
    "v_mfma_f32_16x16x32_bf16 v[36:39], v[192:195], v[164:167], v[36:39]\n"
    "ds_read_b64_tr_b16 v[188:189], v248 offset:96\n"
    "ds_read_b64_tr_b16 v[190:191], v248 offset:3680\n"
    "v_mfma_f32_16x16x32_bf16 v[96:99], v[200:203], v[164:167], v[96:99]\n"
    "v_mfma_f32_16x16x32_bf16 v[48:51], v[172:175], v[164:167], v[48:51]\n"
    "s_waitcnt lgkmcnt(8)\n"
    "v_mfma_f32_16x16x32_bf16 v[108:111], v[56:59], v[76:79], v[108:111]\n"
    "ds_read_b64_tr_b16 v[24:25], v248 offset:128\n"
    "ds_read_b64_tr_b16 v[26:27], v248 offset:3712\n"
    "s_waitcnt lgkmcnt(9)\n"
    "v_mfma_f32_16x16x32_bf16 v[240:243], v[204:207], v[76:79], v[240:243]\n"
    "s_waitcnt lgkmcnt(8)\n"
    "v_mfma_f32_16x16x32_bf16 v[8:11], v[212:215], v[76:79], v[8:11]\n"
    "s_waitcnt lgkmcnt(6)\n"
    "v_mfma_f32_16x16x32_bf16 v[220:223], v[212:215], v[0:3], v[220:223]\n"
    "ds_read_b64_tr_b16 v[104:105], v248 offset:160\n"
    "ds_read_b64_tr_b16 v[106:107], v248 offset:3744\n"
    "v_mfma_f32_16x16x32_bf16 v[44:47], v[204:207], v[0:3], v[44:47]\n"
    "v_mfma_f32_16x16x32_bf16 v[208:211], v[56:59], v[0:3], v[208:211]\n"
    "s_waitcnt lgkmcnt(6)\n"
    "v_mfma_f32_16x16x32_bf16 v[40:43], v[56:59], v[124:127], v[40:43]\n"
    "ds_read_b64_tr_b16 v[196:197], v248 offset:192\n"
    "ds_read_b64_tr_b16 v[198:199], v248 offset:3776\n"
    "v_mfma_f32_16x16x32_bf16 v[132:135], v[204:207], v[124:127], v[132:135]\n"
    "v_mfma_f32_16x16x32_bf16 v[16:19], v[212:215], v[124:127], v[16:19]\n"
    "s_waitcnt lgkmcnt(6)\n"
    "v_mfma_f32_16x16x32_bf16 v[64:67], v[212:215], v[188:191], v[64:67]\n"
    "ds_read_b64_tr_b16 v[116:117], v248 offset:7168\n"
    "ds_read_b64_tr_b16 v[118:119], v248 offset:10752\n"
    "v_mfma_f32_16x16x32_bf16 v[92:95], v[204:207], v[188:191], v[92:95]\n"
    "v_mfma_f32_16x16x32_bf16 v[228:231], v[56:59], v[188:191], v[228:231]\n"
    "s_waitcnt lgkmcnt(6)\n"
    "v_mfma_f32_16x16x32_bf16 v[176:179], v[56:59], v[24:27], v[176:179]\n"
    "ds_read_b64_tr_b16 v[152:153], v248 offset:7200\n"
    "ds_read_b64_tr_b16 v[154:155], v248 offset:10784\n"
    "v_mfma_f32_16x16x32_bf16 v[216:219], v[204:207], v[24:27], v[216:219]\n"
    "ds_read_b128 v[88:91], v253 offset:6144\n"
    "ds_read_b128 v[72:75], v253 offset:8192\n"
    "v_mfma_f32_16x16x32_bf16 v[112:115], v[212:215], v[24:27], v[112:115]\n"
    "ds_read_b128 v[236:239], v253 offset:10240\n"
    "s_waitcnt lgkmcnt(9)\n"
    "v_mfma_f32_16x16x32_bf16 v[160:163], v[212:215], v[104:107], v[160:163]\n"
    "ds_read_b64_tr_b16 v[4:5], v248 offset:7232\n"
    "ds_read_b64_tr_b16 v[6:7], v248 offset:10816\n"
    "v_mfma_f32_16x16x32_bf16 v[60:63], v[204:207], v[104:107], v[60:63]\n"
    "v_mfma_f32_16x16x32_bf16 v[12:15], v[56:59], v[104:107], v[12:15]\n"
    "s_waitcnt lgkmcnt(9)\n"
    "v_mfma_f32_16x16x32_bf16 v[48:51], v[56:59], v[196:199], v[48:51]\n"
    "ds_read_b64_tr_b16 v[164:165], v248 offset:7264\n"
    "ds_read_b64_tr_b16 v[166:167], v248 offset:10848\n"
    "v_mfma_f32_16x16x32_bf16 v[96:99], v[204:207], v[196:199], v[96:99]\n"
    "v_mfma_f32_16x16x32_bf16 v[36:39], v[212:215], v[196:199], v[36:39]\n"
    "s_waitcnt lgkmcnt(4)\n"
    "v_mfma_f32_16x16x32_bf16 v[8:11], v[236:239], v[116:119], v[8:11]\n"
    "ds_read_b64_tr_b16 v[172:173], v248 offset:7296\n"
    "ds_read_b64_tr_b16 v[174:175], v248 offset:10880\n"
    "v_mfma_f32_16x16x32_bf16 v[240:243], v[72:75], v[116:119], v[240:243]\n"
    "v_mfma_f32_16x16x32_bf16 v[108:111], v[88:91], v[116:119], v[108:111]\n"
    "v_mfma_f32_16x16x32_bf16 v[208:211], v[88:91], v[152:155], v[208:211]\n"
    "ds_read_b64_tr_b16 v[200:201], v248 offset:7328\n"
    "ds_read_b64_tr_b16 v[202:203], v248 offset:10912\n"
    "v_mfma_f32_16x16x32_bf16 v[44:47], v[72:75], v[152:155], v[44:47]\n"
    "v_mfma_f32_16x16x32_bf16 v[220:223], v[236:239], v[152:155], v[220:223]\n"
    "s_waitcnt lgkmcnt(6)\n"
    "v_mfma_f32_16x16x32_bf16 v[16:19], v[236:239], v[4:7], v[16:19]\n"
    "ds_read_b64_tr_b16 v[192:193], v248 offset:7360\n"
    "ds_read_b64_tr_b16 v[194:195], v248 offset:10944\n"
    "v_mfma_f32_16x16x32_bf16 v[132:135], v[72:75], v[4:7], v[132:135]\n"
    "v_mfma_f32_16x16x32_bf16 v[40:43], v[88:91], v[4:7], v[40:43]\n"
    "s_waitcnt lgkmcnt(6)\n"
    "v_mfma_f32_16x16x32_bf16 v[228:231], v[88:91], v[164:167], v[228:231]\n"
    "v_mfma_f32_16x16x32_bf16 v[92:95], v[72:75], v[164:167], v[92:95]\n"
    "v_mfma_f32_16x16x32_bf16 v[64:67], v[236:239], v[164:167], v[64:67]\n"
    "s_waitcnt lgkmcnt(0)\n"
    "s_barrier\n"
    "v_mfma_f32_16x16x32_bf16 v[112:115], v[236:239], v[172:175], v[112:115]\n"
    "ds_read_b64_tr_b16 v[76:77], v248 offset:16384\n"
    "ds_read_b64_tr_b16 v[78:79], v248 offset:19968\n"
    "v_mfma_f32_16x16x32_bf16 v[216:219], v[72:75], v[172:175], v[216:219]\n"
    "ds_read_b128 v[0:3], v252 offset:0\n"
    "ds_read_b128 v[124:127], v252 offset:2048\n"
    "v_mfma_f32_16x16x32_bf16 v[176:179], v[88:91], v[172:175], v[176:179]\n"
    "ds_read_b128 v[188:191], v252 offset:4096\n"
    "ds_read_b64_tr_b16 v[24:25], v248 offset:16416\n"
    "v_mfma_f32_16x16x32_bf16 v[12:15], v[88:91], v[200:203], v[12:15]\n"
    "ds_read_b64_tr_b16 v[26:27], v248 offset:20000\n"
    "ds_read_b64_tr_b16 v[104:105], v248 offset:16448\n"
    "v_mfma_f32_16x16x32_bf16 v[60:63], v[72:75], v[200:203], v[60:63]\n"
    "ds_read_b64_tr_b16 v[106:107], v248 offset:20032\n"
    "v_mfma_f32_16x16x32_bf16 v[160:163], v[236:239], v[200:203], v[160:163]\n"
    "v_mfma_f32_16x16x32_bf16 v[36:39], v[236:239], v[192:195], v[36:39]\n"
    "ds_read_b64_tr_b16 v[196:197], v248 offset:16480\n"
    "ds_read_b64_tr_b16 v[198:199], v248 offset:20064\n"
    "v_mfma_f32_16x16x32_bf16 v[96:99], v[72:75], v[192:195], v[96:99]\n"
    "v_mfma_f32_16x16x32_bf16 v[48:51], v[88:91], v[192:195], v[48:51]\n"
    "s_waitcnt lgkmcnt(8)\n"
    "v_mfma_f32_16x16x32_bf16 v[108:111], v[0:3], v[76:79], v[108:111]\n"
    "ds_read_b64_tr_b16 v[56:57], v248 offset:16512\n"
    "ds_read_b64_tr_b16 v[58:59], v248 offset:20096\n"
    "s_waitcnt lgkmcnt(9)\n"
    "v_mfma_f32_16x16x32_bf16 v[240:243], v[124:127], v[76:79], v[240:243]\n"
    "s_waitcnt lgkmcnt(8)\n"
    "v_mfma_f32_16x16x32_bf16 v[8:11], v[188:191], v[76:79], v[8:11]\n"
    "s_waitcnt lgkmcnt(6)\n"
    "v_mfma_f32_16x16x32_bf16 v[220:223], v[188:191], v[24:27], v[220:223]\n"
    "ds_read_b64_tr_b16 v[204:205], v248 offset:16544\n"
    "ds_read_b64_tr_b16 v[206:207], v248 offset:20128\n"
    "v_mfma_f32_16x16x32_bf16 v[44:47], v[124:127], v[24:27], v[44:47]\n"
    "v_mfma_f32_16x16x32_bf16 v[208:211], v[0:3], v[24:27], v[208:211]\n"
    "s_waitcnt lgkmcnt(6)\n"
    "v_mfma_f32_16x16x32_bf16 v[40:43], v[0:3], v[104:107], v[40:43]\n"
    "ds_read_b64_tr_b16 v[212:213], v248 offset:16576\n"
    "ds_read_b64_tr_b16 v[214:215], v248 offset:20160\n"
    "v_mfma_f32_16x16x32_bf16 v[132:135], v[124:127], v[104:107], v[132:135]\n"
    "v_mfma_f32_16x16x32_bf16 v[16:19], v[188:191], v[104:107], v[16:19]\n"
    "s_waitcnt lgkmcnt(6)\n"
    "v_mfma_f32_16x16x32_bf16 v[64:67], v[188:191], v[196:199], v[64:67]\n"
    "ds_read_b64_tr_b16 v[116:117], v248 offset:23552\n"
    "ds_read_b64_tr_b16 v[118:119], v248 offset:27136\n"
    "v_mfma_f32_16x16x32_bf16 v[92:95], v[124:127], v[196:199], v[92:95]\n"
    "v_mfma_f32_16x16x32_bf16 v[228:231], v[0:3], v[196:199], v[228:231]\n"
    "s_waitcnt lgkmcnt(6)\n"
    "v_mfma_f32_16x16x32_bf16 v[176:179], v[0:3], v[56:59], v[176:179]\n"
    "ds_read_b64_tr_b16 v[152:153], v248 offset:23584\n"
    "ds_read_b64_tr_b16 v[154:155], v248 offset:27168\n"
    "v_mfma_f32_16x16x32_bf16 v[216:219], v[124:127], v[56:59], v[216:219]\n"
    "ds_read_b128 v[4:7], v253 offset:0\n"
    "ds_read_b128 v[164:167], v253 offset:2048\n"
    "v_mfma_f32_16x16x32_bf16 v[112:115], v[188:191], v[56:59], v[112:115]\n"
    "ds_read_b128 v[172:175], v253 offset:4096\n"
    "s_waitcnt lgkmcnt(9)\n"
    "v_mfma_f32_16x16x32_bf16 v[160:163], v[188:191], v[204:207], v[160:163]\n"
    "ds_read_b64_tr_b16 v[200:201], v248 offset:23616\n"
    "ds_read_b64_tr_b16 v[202:203], v248 offset:27200\n"
    "v_mfma_f32_16x16x32_bf16 v[60:63], v[124:127], v[204:207], v[60:63]\n"
    "v_mfma_f32_16x16x32_bf16 v[12:15], v[0:3], v[204:207], v[12:15]\n"
    "s_waitcnt lgkmcnt(9)\n"
    "v_mfma_f32_16x16x32_bf16 v[48:51], v[0:3], v[212:215], v[48:51]\n"
    "ds_read_b64_tr_b16 v[192:193], v248 offset:23648\n"
    "ds_read_b64_tr_b16 v[194:195], v248 offset:27232\n"
    "v_mfma_f32_16x16x32_bf16 v[96:99], v[124:127], v[212:215], v[96:99]\n"
    "v_mfma_f32_16x16x32_bf16 v[36:39], v[188:191], v[212:215], v[36:39]\n"
    "s_waitcnt lgkmcnt(4)\n"
    "v_mfma_f32_16x16x32_bf16 v[8:11], v[172:175], v[116:119], v[8:11]\n"
    "ds_read_b64_tr_b16 v[88:89], v248 offset:23680\n"
    "ds_read_b64_tr_b16 v[90:91], v248 offset:27264\n"
    "v_mfma_f32_16x16x32_bf16 v[240:243], v[164:167], v[116:119], v[240:243]\n"
    "v_mfma_f32_16x16x32_bf16 v[108:111], v[4:7], v[116:119], v[108:111]\n"
    "v_mfma_f32_16x16x32_bf16 v[208:211], v[4:7], v[152:155], v[208:211]\n"
    "ds_read_b64_tr_b16 v[72:73], v248 offset:23712\n"
    "ds_read_b64_tr_b16 v[74:75], v248 offset:27296\n"
    "v_mfma_f32_16x16x32_bf16 v[44:47], v[164:167], v[152:155], v[44:47]\n"
    "v_mfma_f32_16x16x32_bf16 v[220:223], v[172:175], v[152:155], v[220:223]\n"
    "s_nop 2\n"
    "v_cvt_pk_bf16_f32 v108, v108, v109\n"
    "v_cvt_pk_bf16_f32 v109, v110, v111\n"
    "s_waitcnt lgkmcnt(6)\n"
    "v_mfma_f32_16x16x32_bf16 v[16:19], v[172:175], v[200:203], v[16:19]\n"
    "ds_read_b64_tr_b16 v[236:237], v248 offset:23744\n"
    "ds_read_b64_tr_b16 v[238:239], v248 offset:27328\n"
    "v_mfma_f32_16x16x32_bf16 v[132:135], v[164:167], v[200:203], v[132:135]\n"
    "v_cvt_pk_bf16_f32 v110, v240, v241\n"
    "v_cvt_pk_bf16_f32 v111, v242, v243\n"
    "v_mfma_f32_16x16x32_bf16 v[40:43], v[4:7], v[200:203], v[40:43]\n"
    "v_cvt_pk_bf16_f32 v8, v8, v9\n"
    "v_cvt_pk_bf16_f32 v9, v10, v11\n"
    "s_waitcnt lgkmcnt(6)\n"
    "v_mfma_f32_16x16x32_bf16 v[228:231], v[4:7], v[192:195], v[228:231]\n"
    "v_cvt_pk_bf16_f32 v208, v208, v209\n"
    "v_cvt_pk_bf16_f32 v209, v210, v211\n"
    "v_mfma_f32_16x16x32_bf16 v[92:95], v[164:167], v[192:195], v[92:95]\n"
    "v_cvt_pk_bf16_f32 v210, v44, v45\n"
    "v_cvt_pk_bf16_f32 v211, v46, v47\n"
    "v_mfma_f32_16x16x32_bf16 v[64:67], v[172:175], v[192:195], v[64:67]\n"
    "v_cvt_pk_bf16_f32 v220, v220, v221\n"
    "v_cvt_pk_bf16_f32 v221, v222, v223\n"
    "s_waitcnt lgkmcnt(0)\n"
    "s_barrier\n"
    "v_mfma_f32_16x16x32_bf16 v[112:115], v[172:175], v[88:91], v[112:115]\n"
    "ds_read_b128 v[76:79], v249 offset:12288\n"
    "ds_read_b128 v[24:27], v249 offset:13312\n"
    "v_mfma_f32_16x16x32_bf16 v[216:219], v[164:167], v[88:91], v[216:219]\n"
    "ds_read_b128 v[104:107], v249 offset:14336\n"
    "ds_read_b128 v[196:199], v249 offset:15360\n"
    "v_mfma_f32_16x16x32_bf16 v[176:179], v[4:7], v[88:91], v[176:179]\n"
    "ds_read_b128 v[56:59], v249 offset:16384\n"
    "ds_read_b128 v[204:207], v249 offset:17408\n"
    "v_mfma_f32_16x16x32_bf16 v[12:15], v[4:7], v[72:75], v[12:15]\n"
    "ds_read_b128 v[212:215], v249 offset:18432\n"
    "ds_read_b128 v[0:3], v249 offset:19456\n"
    "v_mfma_f32_16x16x32_bf16 v[60:63], v[164:167], v[72:75], v[60:63]\n"
    "ds_read_b128 v[124:127], v249 offset:20480\n"
    "ds_read_b64 v[188:189], v249 offset:21504\n"
    "v_mfma_f32_16x16x32_bf16 v[160:163], v[172:175], v[72:75], v[160:163]\n"
    "ds_read_b64 v[190:191], v249 offset:23040\n"
    "ds_read_b64 v[116:117], v249 offset:21512\n"
    "v_mfma_f32_16x16x32_bf16 v[36:39], v[172:175], v[236:239], v[36:39]\n"
    "ds_read_b64 v[118:119], v249 offset:23048\n"
    "ds_read_b64 v[152:153], v250 offset:22528\n"
    "v_mfma_f32_16x16x32_bf16 v[96:99], v[164:167], v[236:239], v[96:99]\n"
    "ds_read_b64 v[154:155], v250 offset:24064\n"
    "v_cvt_pk_bf16_f32 v40, v40, v41\n"
    "v_cvt_pk_bf16_f32 v41, v42, v43\n"
    "v_mfma_f32_16x16x32_bf16 v[48:51], v[4:7], v[236:239], v[48:51]\n"
    "v_cvt_pk_bf16_f32 v42, v132, v133\n"
    "v_cvt_pk_bf16_f32 v43, v134, v135\n"
    "s_waitcnt lgkmcnt(14)\n"
    "v_mfma_f32_16x16x32_bf16 v[240:243], v[80:83], v[76:79], 0\n"
    "v_cvt_pk_bf16_f32 v16, v16, v17\n"
    "v_cvt_pk_bf16_f32 v17, v18, v19\n"
    "s_waitcnt lgkmcnt(13)\n"
    "v_mfma_f32_16x16x32_bf16 v[200:203], v[80:83], v[24:27], 0\n"
    "v_cvt_pk_bf16_f32 v228, v228, v229\n"
    "v_cvt_pk_bf16_f32 v229, v230, v231\n"
    "s_waitcnt lgkmcnt(12)\n"
    "v_mfma_f32_16x16x32_bf16 v[44:47], v[80:83], v[104:107], 0\n"
    "v_cvt_pk_bf16_f32 v230, v92, v93\n"
    "v_cvt_pk_bf16_f32 v231, v94, v95\n"
    "s_waitcnt lgkmcnt(9)\n"
    "v_mfma_f32_16x16x32_bf16 v[44:47], v[180:183], v[204:207], v[44:47]\n"
    "v_cvt_pk_bf16_f32 v64, v64, v65\n"
    "v_cvt_pk_bf16_f32 v65, v66, v67\n"
    "v_mfma_f32_16x16x32_bf16 v[200:203], v[180:183], v[56:59], v[200:203]\n"
    "v_cvt_pk_bf16_f32 v176, v176, v177\n"
    "v_cvt_pk_bf16_f32 v177, v178, v179\n"
    "v_mfma_f32_16x16x32_bf16 v[240:243], v[180:183], v[196:199], v[240:243]\n"
    "v_cvt_pk_bf16_f32 v178, v216, v217\n"
    "v_cvt_pk_bf16_f32 v179, v218, v219\n"
    "s_waitcnt lgkmcnt(8)\n"
    "v_mfma_f32_16x16x32_bf16 v[240:243], v[108:111], v[212:215], v[240:243]\n"
    "v_cvt_pk_bf16_f32 v112, v112, v113\n"
    "v_cvt_pk_bf16_f32 v113, v114, v115\n"
    "s_waitcnt lgkmcnt(7)\n"
    "v_mfma_f32_16x16x32_bf16 v[200:203], v[108:111], v[0:3], v[200:203]\n"
    "v_cvt_pk_bf16_f32 v12, v12, v13\n"
    "v_cvt_pk_bf16_f32 v13, v14, v15\n"
    "s_waitcnt lgkmcnt(6)\n"
    "v_mfma_f32_16x16x32_bf16 v[44:47], v[108:111], v[124:127], v[44:47]\n"
    "v_cvt_pk_bf16_f32 v14, v60, v61\n"
    "v_cvt_pk_bf16_f32 v15, v62, v63\n"
    "s_waitcnt lgkmcnt(0)\n"
    "v_mfma_f32_16x16x32_bf16 v[44:47], v[68:71], v[152:155], v[44:47]\n"
    "v_cvt_pk_bf16_f32 v160, v160, v161\n"
    "v_cvt_pk_bf16_f32 v161, v162, v163\n"
    "v_mfma_f32_16x16x32_bf16 v[200:203], v[68:71], v[116:119], v[200:203]\n"
    "v_cvt_pk_bf16_f32 v48, v48, v49\n"
    "v_cvt_pk_bf16_f32 v49, v50, v51\n"
    "v_mfma_f32_16x16x32_bf16 v[240:243], v[68:71], v[188:191], v[240:243]\n"
    "v_cvt_pk_bf16_f32 v50, v96, v97\n"
    "v_cvt_pk_bf16_f32 v51, v98, v99\n"
    "v_mfma_f32_16x16x32_bf16 v[192:195], v[136:139], v[76:79], 0\n"
    "v_cvt_pk_bf16_f32 v36, v36, v37\n"
    "v_cvt_pk_bf16_f32 v37, v38, v39\n"
    "v_mfma_f32_16x16x32_bf16 v[88:91], v[136:139], v[24:27], 0\n"
    "v_mfma_f32_16x16x32_bf16 v[72:75], v[136:139], v[104:107], 0\n"
    "s_barrier\n"
    "v_mfma_f32_16x16x32_bf16 v[72:75], v[144:147], v[204:207], v[72:75]\n"
    "v_mfma_f32_16x16x32_bf16 v[88:91], v[144:147], v[56:59], v[88:91]\n"
    "v_mfma_f32_16x16x32_bf16 v[192:195], v[144:147], v[196:199], v[192:195]\n"
    "v_mfma_f32_16x16x32_bf16 v[192:195], v[208:211], v[212:215], v[192:195]\n"
    "v_mfma_f32_16x16x32_bf16 v[88:91], v[208:211], v[0:3], v[88:91]\n"
    "v_mfma_f32_16x16x32_bf16 v[72:75], v[208:211], v[124:127], v[72:75]\n"
    "v_mfma_f32_16x16x32_bf16 v[72:75], v[140:143], v[152:155], v[72:75]\n"
    "v_mfma_f32_16x16x32_bf16 v[88:91], v[140:143], v[116:119], v[88:91]\n"
    "v_mfma_f32_16x16x32_bf16 v[192:195], v[140:143], v[188:191], v[192:195]\n"
    "v_mfma_f32_16x16x32_bf16 v[132:135], v[156:159], v[76:79], 0\n"
    "v_mfma_f32_16x16x32_bf16 v[236:239], v[156:159], v[24:27], 0\n"
    "v_mfma_f32_16x16x32_bf16 v[4:7], v[156:159], v[104:107], 0\n"
    "v_mfma_f32_16x16x32_bf16 v[4:7], v[148:151], v[204:207], v[4:7]\n"
    "v_mfma_f32_16x16x32_bf16 v[236:239], v[148:151], v[56:59], v[236:239]\n"
    "v_mfma_f32_16x16x32_bf16 v[132:135], v[148:151], v[196:199], v[132:135]\n"
    "v_mfma_f32_16x16x32_bf16 v[132:135], v[40:43], v[212:215], v[132:135]\n"
    "v_mfma_f32_16x16x32_bf16 v[236:239], v[40:43], v[0:3], v[236:239]\n"
    "v_mfma_f32_16x16x32_bf16 v[4:7], v[40:43], v[124:127], v[4:7]\n"
    "v_mfma_f32_16x16x32_bf16 v[4:7], v[28:31], v[152:155], v[4:7]\n"
    "v_mfma_f32_16x16x32_bf16 v[236:239], v[28:31], v[116:119], v[236:239]\n"
    "v_mfma_f32_16x16x32_bf16 v[132:135], v[28:31], v[188:191], v[132:135]\n"
    "v_mfma_f32_16x16x32_bf16 v[164:167], v[32:35], v[76:79], 0\n"
    "v_mfma_f32_16x16x32_bf16 v[172:175], v[32:35], v[24:27], 0\n"
    "v_mfma_f32_16x16x32_bf16 v[92:95], v[32:35], v[104:107], 0\n"
    "v_mfma_f32_16x16x32_bf16 v[92:95], v[168:171], v[204:207], v[92:95]\n"
    "v_mfma_f32_16x16x32_bf16 v[172:175], v[168:171], v[56:59], v[172:175]\n"
    "v_mfma_f32_16x16x32_bf16 v[164:167], v[168:171], v[196:199], v[164:167]\n"
    "s_barrier\n"
    "v_mfma_f32_16x16x32_bf16 v[164:167], v[228:231], v[212:215], v[164:167]\n"
    "ds_read_b128 v[80:83], v249 offset:0\n"
    "ds_read_b128 v[216:219], v249 offset:1024\n"
    "v_mfma_f32_16x16x32_bf16 v[172:175], v[228:231], v[0:3], v[172:175]\n"
    "ds_read_b128 v[180:183], v249 offset:2048\n"
    "ds_read_b64 v[60:61], v249 offset:3072\n"
    "v_mfma_f32_16x16x32_bf16 v[92:95], v[228:231], v[124:127], v[92:95]\n"
    "ds_read_b64 v[62:63], v249 offset:4608\n"
    "ds_read_b64 v[108:109], v249 offset:3080\n"
    "v_mfma_f32_16x16x32_bf16 v[92:95], v[100:103], v[152:155], v[92:95]\n"
    "ds_read_b64 v[110:111], v249 offset:4616\n"
    "ds_read_b64 v[96:97], v250 offset:4096\n"
    "v_mfma_f32_16x16x32_bf16 v[172:175], v[100:103], v[116:119], v[172:175]\n"
    "ds_read_b64 v[98:99], v250 offset:5632\n"
    "v_mfma_f32_16x16x32_bf16 v[164:167], v[100:103], v[188:191], v[164:167]\n"
    "v_mfma_f32_16x16x32_bf16 v[68:71], v[84:87], v[76:79], 0\n"
    "v_mfma_f32_16x16x32_bf16 v[136:139], v[84:87], v[24:27], 0\n"
    "v_mfma_f32_16x16x32_bf16 v[144:147], v[84:87], v[104:107], 0\n"
    "v_mfma_f32_16x16x32_bf16 v[144:147], v[20:23], v[204:207], v[144:147]\n"
    "v_mfma_f32_16x16x32_bf16 v[136:139], v[20:23], v[56:59], v[136:139]\n"
    "v_mfma_f32_16x16x32_bf16 v[68:71], v[20:23], v[196:199], v[68:71]\n"
    "v_mfma_f32_16x16x32_bf16 v[68:71], v[176:179], v[212:215], v[68:71]\n"
    "v_mfma_f32_16x16x32_bf16 v[136:139], v[176:179], v[0:3], v[136:139]\n"
    "v_mfma_f32_16x16x32_bf16 v[144:147], v[176:179], v[124:127], v[144:147]\n"
    "v_mfma_f32_16x16x32_bf16 v[144:147], v[232:235], v[152:155], v[144:147]\n"
    "v_mfma_f32_16x16x32_bf16 v[136:139], v[232:235], v[116:119], v[136:139]\n"
    "v_mfma_f32_16x16x32_bf16 v[68:71], v[232:235], v[188:191], v[68:71]\n"
    "v_mfma_f32_16x16x32_bf16 v[208:211], v[184:187], v[76:79], 0\n"
    "v_mfma_f32_16x16x32_bf16 v[140:143], v[184:187], v[24:27], 0\n"
    "v_mfma_f32_16x16x32_bf16 v[156:159], v[184:187], v[104:107], 0\n"
    "v_mfma_f32_16x16x32_bf16 v[156:159], v[224:227], v[204:207], v[156:159]\n"
    "v_mfma_f32_16x16x32_bf16 v[140:143], v[224:227], v[56:59], v[140:143]\n"
    "v_mfma_f32_16x16x32_bf16 v[208:211], v[224:227], v[196:199], v[208:211]\n"
    "v_mfma_f32_16x16x32_bf16 v[208:211], v[12:15], v[212:215], v[208:211]\n"
    "v_mfma_f32_16x16x32_bf16 v[140:143], v[12:15], v[0:3], v[140:143]\n"
    "v_mfma_f32_16x16x32_bf16 v[156:159], v[12:15], v[124:127], v[156:159]\n"
    "v_mfma_f32_16x16x32_bf16 v[156:159], v[244:247], v[152:155], v[156:159]\n"
    "v_mfma_f32_16x16x32_bf16 v[140:143], v[244:247], v[116:119], v[140:143]\n"
    "v_mfma_f32_16x16x32_bf16 v[208:211], v[244:247], v[188:191], v[208:211]\n"
    "v_mfma_f32_16x16x32_bf16 v[148:151], v[120:123], v[76:79], 0\n"
    "v_mfma_f32_16x16x32_bf16 v[40:43], v[120:123], v[24:27], 0\n"
    "v_mfma_f32_16x16x32_bf16 v[28:31], v[120:123], v[104:107], 0\n"
    "s_waitcnt lgkmcnt(0)\n"
    "s_barrier\n"
    "v_mfma_f32_16x16x32_bf16 v[28:31], v[52:55], v[204:207], v[28:31]\n"
    "ds_read_b128 v[32:35], v252 offset:6144\n"
    "ds_read_b128 v[168:171], v252 offset:8192\n"
    "v_mfma_f32_16x16x32_bf16 v[40:43], v[52:55], v[56:59], v[40:43]\n"
    "ds_read_b128 v[228:231], v252 offset:10240\n"
    "ds_read_b64_tr_b16 v[100:101], v248 offset:32768\n"
    "v_mfma_f32_16x16x32_bf16 v[148:151], v[52:55], v[196:199], v[148:151]\n"
    "ds_read_b64_tr_b16 v[102:103], v248 offset:36352\n"
    "ds_read_b64_tr_b16 v[84:85], v248 offset:32800\n"
    "v_mfma_f32_16x16x32_bf16 v[148:151], v[48:51], v[212:215], v[148:151]\n"
    "ds_read_b64_tr_b16 v[86:87], v248 offset:36384\n"
    "ds_read_b128 v[20:23], v253 offset:6144\n"
    "v_mfma_f32_16x16x32_bf16 v[40:43], v[48:51], v[0:3], v[40:43]\n"
    "ds_read_b128 v[176:179], v253 offset:8192\n"
    "ds_read_b128 v[232:235], v253 offset:10240\n"
    "v_mfma_f32_16x16x32_bf16 v[28:31], v[48:51], v[124:127], v[28:31]\n"
    "ds_read_b64_tr_b16 v[184:185], v248 offset:39936\n"
    "ds_read_b64_tr_b16 v[186:187], v248 offset:43520\n"
    "v_mfma_f32_16x16x32_bf16 v[28:31], v[128:131], v[152:155], v[28:31]\n"
    "ds_read_b64_tr_b16 v[224:225], v248 offset:39968\n"
    "ds_read_b64_tr_b16 v[226:227], v248 offset:43552\n"
    "v_mfma_f32_16x16x32_bf16 v[40:43], v[128:131], v[116:119], v[40:43]\n"
    "v_mfma_f32_16x16x32_bf16 v[148:151], v[128:131], v[188:191], v[148:151]\n"
    "v_lshlrev_b32 v12, 4, v255\n"
    "s_lshl_b32 s60, s28, 11\n"
    "s_add_u32 s60, s60, s62\n"
    "s_waitcnt lgkmcnt(9)\n"
    "v_mfma_f32_16x16x32_bf16 v[244:247], v[32:35], v[100:103], 0\n"
    "v_mfma_f32_16x16x32_bf16 v[120:123], v[168:171], v[100:103], 0\n"
    "v_mfma_f32_16x16x32_bf16 v[52:55], v[228:231], v[100:103], 0\n"
    "s_waitcnt lgkmcnt(7)\n"
    "v_mfma_f32_16x16x32_bf16 v[76:79], v[228:231], v[84:87], 0\n"
    "v_mfma_f32_16x16x32_bf16 v[128:131], v[168:171], v[84:87], 0\n"
    "v_mfma_f32_16x16x32_bf16 v[48:51], v[32:35], v[84:87], 0\n"
    "s_waitcnt lgkmcnt(2)\n"
    "v_mfma_f32_16x16x32_bf16 v[244:247], v[20:23], v[184:187], v[244:247]\n"
    "ds_read_b128 v[24:27], v252 offset:12288\n"
    "ds_read_b128 v[104:107], v252 offset:14336\n"
    "ds_read_b128 v[196:199], v252 offset:16384\n"
    "v_mfma_f32_16x16x32_bf16 v[120:123], v[176:179], v[184:187], v[120:123]\n"
    "ds_read_b64_tr_b16 v[56:57], v248 offset:49152\n"
    "ds_read_b64_tr_b16 v[58:59], v248 offset:52736\n"
    "ds_read_b64_tr_b16 v[204:205], v248 offset:49184\n"
    "v_mfma_f32_16x16x32_bf16 v[52:55], v[232:235], v[184:187], v[52:55]\n"
    "ds_read_b64_tr_b16 v[206:207], v248 offset:52768\n"
    "s_waitcnt lgkmcnt(7)\n"
    "v_mfma_f32_16x16x32_bf16 v[76:79], v[232:235], v[224:227], v[76:79]\n"
    "v_mfma_f32_16x16x32_bf16 v[128:131], v[176:179], v[224:227], v[128:131]\n"
    "v_mfma_f32_16x16x32_bf16 v[48:51], v[20:23], v[224:227], v[48:51]\n"
    "s_waitcnt lgkmcnt(2)\n"
    "v_mfma_f32_16x16x32_bf16 v[244:247], v[24:27], v[56:59], v[244:247]\n"
    "ds_read_b128 v[212:215], v253 offset:12288\n"
    "ds_read_b128 v[0:3], v253 offset:14336\n"
    "ds_read_b128 v[124:127], v253 offset:16384\n"
    "v_mfma_f32_16x16x32_bf16 v[120:123], v[104:107], v[56:59], v[120:123]\n"
    "ds_read_b64_tr_b16 v[188:189], v248 offset:56320\n"
    "ds_read_b64_tr_b16 v[190:191], v248 offset:59904\n"
    "ds_read_b64_tr_b16 v[116:117], v248 offset:56352\n"
    "v_mfma_f32_16x16x32_bf16 v[52:55], v[196:199], v[56:59], v[52:55]\n"
    "ds_read_b64_tr_b16 v[118:119], v248 offset:59936\n"
    "s_waitcnt lgkmcnt(7)\n"
    "v_mfma_f32_16x16x32_bf16 v[76:79], v[196:199], v[204:207], v[76:79]\n"
    "v_mfma_f32_16x16x32_bf16 v[128:131], v[104:107], v[204:207], v[128:131]\n"
    "v_mfma_f32_16x16x32_bf16 v[48:51], v[24:27], v[204:207], v[48:51]\n"
    "s_waitcnt lgkmcnt(2)\n"
    "v_mfma_f32_16x16x32_bf16 v[244:247], v[212:215], v[188:191], v[244:247]\n"
    "ds_read_b128 v[152:155], v252 offset:18432\n"
    "ds_read_b128 v[100:103], v252 offset:20480\n"
    "ds_read_b128 v[84:87], v252 offset:22528\n"
    "v_mfma_f32_16x16x32_bf16 v[120:123], v[0:3], v[188:191], v[120:123]\n"
    "ds_read_b64_tr_b16 v[32:33], v248 offset:0\n"
    "ds_read_b64_tr_b16 v[34:35], v248 offset:3584\n"
    "ds_read_b64_tr_b16 v[168:169], v248 offset:32\n"
    "v_mfma_f32_16x16x32_bf16 v[52:55], v[124:127], v[188:191], v[52:55]\n"
    "ds_read_b64_tr_b16 v[170:171], v248 offset:3616\n"
    "s_waitcnt lgkmcnt(7)\n"
    "v_mfma_f32_16x16x32_bf16 v[76:79], v[124:127], v[116:119], v[76:79]\n"
    "v_mfma_f32_16x16x32_bf16 v[128:131], v[0:3], v[116:119], v[128:131]\n"
    "v_mfma_f32_16x16x32_bf16 v[48:51], v[212:215], v[116:119], v[48:51]\n"
    "s_waitcnt lgkmcnt(2)\n"
    "v_mfma_f32_16x16x32_bf16 v[244:247], v[152:155], v[32:35], v[244:247]\n"
    "ds_read_b128 v[228:231], v253 offset:18432\n"
    "ds_read_b128 v[184:187], v253 offset:20480\n"
    "ds_read_b128 v[224:227], v253 offset:22528\n"
    "v_mfma_f32_16x16x32_bf16 v[120:123], v[100:103], v[32:35], v[120:123]\n"
    "ds_read_b64_tr_b16 v[20:21], v248 offset:7168\n"
    "ds_read_b64_tr_b16 v[22:23], v248 offset:10752\n"
    "ds_read_b64_tr_b16 v[176:177], v248 offset:7200\n"
    "v_mfma_f32_16x16x32_bf16 v[52:55], v[84:87], v[32:35], v[52:55]\n"
    "ds_read_b64_tr_b16 v[178:179], v248 offset:10784\n"
    "s_barrier\n"
    "s_waitcnt lgkmcnt(7)\n"
    "v_mfma_f32_16x16x32_bf16 v[76:79], v[84:87], v[168:171], v[76:79]\n"
    "v_mfma_f32_16x16x32_bf16 v[128:131], v[100:103], v[168:171], v[128:131]\n"
    "v_mfma_f32_16x16x32_bf16 v[48:51], v[152:155], v[168:171], v[48:51]\n"
    "s_waitcnt lgkmcnt(2)\n"
    "v_mfma_f32_16x16x32_bf16 v[244:247], v[228:231], v[20:23], v[244:247]\n"
    "ds_read_b128 v[232:235], v252 offset:0\n"
    "ds_read_b128 v[56:59], v252 offset:2048\n"
    "ds_read_b128 v[204:207], v252 offset:4096\n"
    "v_mfma_f32_16x16x32_bf16 v[120:123], v[184:187], v[20:23], v[120:123]\n"
    "ds_read_b64_tr_b16 v[24:25], v248 offset:16384\n"
    "ds_read_b64_tr_b16 v[26:27], v248 offset:19968\n"
    "ds_read_b64_tr_b16 v[104:105], v248 offset:16416\n"
    "v_mfma_f32_16x16x32_bf16 v[52:55], v[224:227], v[20:23], v[52:55]\n"
    "ds_read_b64_tr_b16 v[106:107], v248 offset:20000\n"
    "s_waitcnt lgkmcnt(7)\n"
    "v_mfma_f32_16x16x32_bf16 v[76:79], v[224:227], v[176:179], v[76:79]\n"
    "v_mfma_f32_16x16x32_bf16 v[128:131], v[184:187], v[176:179], v[128:131]\n"
    "v_mfma_f32_16x16x32_bf16 v[48:51], v[228:231], v[176:179], v[48:51]\n"
    "s_waitcnt lgkmcnt(2)\n"
    "v_mfma_f32_16x16x32_bf16 v[244:247], v[232:235], v[24:27], v[244:247]\n"
    "ds_read_b128 v[196:199], v253 offset:0\n"
    "ds_read_b128 v[188:191], v253 offset:2048\n"
    "ds_read_b128 v[116:119], v253 offset:4096\n"
    "v_mfma_f32_16x16x32_bf16 v[120:123], v[56:59], v[24:27], v[120:123]\n"
    "ds_read_b64_tr_b16 v[212:213], v248 offset:23552\n"
    "ds_read_b64_tr_b16 v[214:215], v248 offset:27136\n"
    "ds_read_b64_tr_b16 v[0:1], v248 offset:23584\n"
    "v_mfma_f32_16x16x32_bf16 v[52:55], v[204:207], v[24:27], v[52:55]\n"
    "ds_read_b64_tr_b16 v[2:3], v248 offset:27168\n"
    "s_waitcnt lgkmcnt(7)\n"
    "v_mfma_f32_16x16x32_bf16 v[76:79], v[204:207], v[104:107], v[76:79]\n"
    "v_mfma_f32_16x16x32_bf16 v[128:131], v[56:59], v[104:107], v[128:131]\n"
    "v_mfma_f32_16x16x32_bf16 v[48:51], v[232:235], v[104:107], v[48:51]\n"
    "s_waitcnt lgkmcnt(2)\n"
    "v_mfma_f32_16x16x32_bf16 v[244:247], v[196:199], v[212:215], v[244:247]\n"
    "ds_read_b128 v[124:127], v252 offset:6144\n"
    "ds_read_b128 v[32:35], v252 offset:8192\n"
    "ds_read_b128 v[168:171], v252 offset:10240\n"
    "v_mfma_f32_16x16x32_bf16 v[120:123], v[188:191], v[212:215], v[120:123]\n"
    "ds_read_b64_tr_b16 v[152:153], v248 offset:32832\n"
    "ds_read_b64_tr_b16 v[154:155], v248 offset:36416\n"
    "ds_read_b64_tr_b16 v[100:101], v248 offset:32864\n"
    "v_mfma_f32_16x16x32_bf16 v[52:55], v[116:119], v[212:215], v[52:55]\n"
    "ds_read_b64_tr_b16 v[102:103], v248 offset:36448\n"
    "s_waitcnt lgkmcnt(7)\n"
    "v_mfma_f32_16x16x32_bf16 v[76:79], v[116:119], v[0:3], v[76:79]\n"
    "v_mfma_f32_16x16x32_bf16 v[128:131], v[188:191], v[0:3], v[128:131]\n"
    "v_mfma_f32_16x16x32_bf16 v[48:51], v[196:199], v[0:3], v[48:51]\n"
    "v_cvt_pk_bf16_f32 v244, v244, v245\n"
    "v_cvt_pk_bf16_f32 v245, v246, v247\n"
    "v_cvt_pk_bf16_f32 v246, v120, v121\n"
    "v_cvt_pk_bf16_f32 v247, v122, v123\n"
    "s_waitcnt lgkmcnt(2)\n"
    "v_mfma_f32_16x16x32_bf16 v[84:87], v[124:127], v[152:155], 0\n"
    "ds_read_b128 v[228:231], v253 offset:6144\n"
    "ds_read_b128 v[184:187], v253 offset:8192\n"
    "ds_read_b128 v[224:227], v253 offset:10240\n"
    "v_mfma_f32_16x16x32_bf16 v[20:23], v[32:35], v[152:155], 0\n"
    "ds_read_b64_tr_b16 v[24:25], v248 offset:40000\n"
    "ds_read_b64_tr_b16 v[26:27], v248 offset:43584\n"
    "ds_read_b64_tr_b16 v[104:105], v248 offset:40032\n"
    "v_mfma_f32_16x16x32_bf16 v[176:179], v[168:171], v[152:155], 0\n"
    "ds_read_b64_tr_b16 v[106:107], v248 offset:43616\n"
    "v_cvt_pk_bf16_f32 v10, v52, v53\n"
    "v_cvt_pk_bf16_f32 v11, v54, v55\n"
    "v_mfma_f32_16x16x32_bf16 v[44:47], v[244:247], v[180:183], v[44:47]\n"
    "v_cvt_pk_bf16_f32 v48, v48, v49\n"
    "v_cvt_pk_bf16_f32 v49, v50, v51\n"
    "v_cvt_pk_bf16_f32 v50, v128, v129\n"
    "v_cvt_pk_bf16_f32 v51, v130, v131\n"
    "v_mfma_f32_16x16x32_bf16 v[200:203], v[244:247], v[216:219], v[200:203]\n"
    "v_cvt_pk_bf16_f32 v222, v76, v77\n"
    "v_cvt_pk_bf16_f32 v223, v78, v79\n"
    "v_mfma_f32_16x16x32_bf16 v[240:243], v[244:247], v[80:83], v[240:243]\n"
    "v_mfma_f32_16x16x32_bf16 v[240:243], v[8:11], v[60:63], v[240:243]\n"
    "v_mfma_f32_16x16x32_bf16 v[200:203], v[8:11], v[108:111], v[200:203]\n"
    "v_mfma_f32_16x16x32_bf16 v[44:47], v[8:11], v[96:99], v[44:47]\n"
    "v_mfma_f32_16x16x32_bf16 v[72:75], v[48:51], v[180:183], v[72:75]\n"
    "v_mfma_f32_16x16x32_bf16 v[88:91], v[48:51], v[216:219], v[88:91]\n"
    "v_mfma_f32_16x16x32_bf16 v[192:195], v[48:51], v[80:83], v[192:195]\n"
    "s_nop 2\n"
    "v_cvt_pk_bf16_f32 v240, v240, v241\n"
    "s_nop 0\n"
    "v_cvt_pk_bf16_f32 v241, v44, s78\n"
    "v_cvt_pk_bf16_f32 v242, v242, v243\n"
    "v_mfma_f32_16x16x32_bf16 v[192:195], v[220:223], v[60:63], v[192:195]\n"
    "v_cvt_pk_bf16_f32 v243, v45, s79\n"
    "s_lshl_b32 s66, s70, 13\n"
    "s_add_u32 s66, s66, s60\n"
    "buffer_store_dwordx4 v[240:243], v12, s[36:39], s66 offen offset:0 sc1\n"
    "v_cvt_pk_bf16_f32 v200, v200, v201\n"
    "v_mfma_f32_16x16x32_bf16 v[88:91], v[220:223], v[108:111], v[88:91]\n"
    "v_cvt_pk_bf16_f32 v201, v46, s78\n"
    "v_cvt_pk_bf16_f32 v202, v202, v203\n"
    "v_cvt_pk_bf16_f32 v203, v47, s79\n"
    "v_mfma_f32_16x16x32_bf16 v[72:75], v[220:223], v[96:99], v[72:75]\n"
    "s_waitcnt lgkmcnt(7)\n"
    "v_mfma_f32_16x16x32_bf16 v[204:207], v[168:171], v[100:103], 0\n"
    "v_mfma_f32_16x16x32_bf16 v[56:59], v[32:35], v[100:103], 0\n"
    "v_mfma_f32_16x16x32_bf16 v[232:235], v[124:127], v[100:103], 0\n"
    "buffer_store_dwordx4 v[200:203], v12, s[36:39], s66 offen offset:1024 sc1\n"
    "v_cvt_pk_bf16_f32 v192, v192, v193\n"
    "s_nop 1\n"
    "v_cvt_pk_bf16_f32 v193, v72, s78\n"
    "s_waitcnt lgkmcnt(2)\n"
    "v_mfma_f32_16x16x32_bf16 v[84:87], v[228:231], v[24:27], v[84:87]\n"
    "ds_read_b128 v[212:215], v252 offset:12288\n"
    "ds_read_b128 v[120:123], v252 offset:14336\n"
    "ds_read_b128 v[0:3], v252 offset:16384\n"
    "v_mfma_f32_16x16x32_bf16 v[20:23], v[184:187], v[24:27], v[20:23]\n"
    "ds_read_b64_tr_b16 v[196:197], v248 offset:49216\n"
    "ds_read_b64_tr_b16 v[198:199], v248 offset:52800\n"
    "ds_read_b64_tr_b16 v[188:189], v248 offset:49248\n"
    "v_mfma_f32_16x16x32_bf16 v[176:179], v[224:227], v[24:27], v[176:179]\n"
    "ds_read_b64_tr_b16 v[190:191], v248 offset:52832\n"
    "v_cvt_pk_bf16_f32 v194, v194, v195\n"
    "v_cvt_pk_bf16_f32 v195, v73, s79\n"
    "s_waitcnt lgkmcnt(7)\n"
    "v_mfma_f32_16x16x32_bf16 v[204:207], v[224:227], v[104:107], v[204:207]\n"
    "v_mfma_f32_16x16x32_bf16 v[56:59], v[184:187], v[104:107], v[56:59]\n"
    "v_mfma_f32_16x16x32_bf16 v[232:235], v[228:231], v[104:107], v[232:235]\n"
    "s_lshl_b32 s66, s71, 13\n"
    "s_add_u32 s66, s66, s60\n"
    "buffer_store_dwordx4 v[192:195], v12, s[36:39], s66 offen offset:0 sc1\n"
    "v_cvt_pk_bf16_f32 v88, v88, v89\n"
    "v_cvt_pk_bf16_f32 v89, v74, s78\n"
    "s_waitcnt lgkmcnt(2)\n"
    "v_mfma_f32_16x16x32_bf16 v[84:87], v[212:215], v[196:199], v[84:87]\n"
    "ds_read_b128 v[76:79], v253 offset:12288\n"
    "ds_read_b128 v[244:247], v253 offset:14336\n"
    "ds_read_b128 v[8:11], v253 offset:16384\n"
    "v_mfma_f32_16x16x32_bf16 v[20:23], v[120:123], v[196:199], v[20:23]\n"
    "ds_read_b64_tr_b16 v[48:49], v248 offset:56384\n"
    "ds_read_b64_tr_b16 v[50:51], v248 offset:59968\n"
    "ds_read_b64_tr_b16 v[220:221], v248 offset:56416\n"
    "v_mfma_f32_16x16x32_bf16 v[176:179], v[0:3], v[196:199], v[176:179]\n"
    "ds_read_b64_tr_b16 v[222:223], v248 offset:60000\n"
    "v_cvt_pk_bf16_f32 v90, v90, v91\n"
    "v_cvt_pk_bf16_f32 v91, v75, s79\n"
    "s_waitcnt lgkmcnt(7)\n"
    "v_mfma_f32_16x16x32_bf16 v[204:207], v[0:3], v[188:191], v[204:207]\n"
    "v_and_b32 v128, 15, v255\n"
    "v_lshrrev_b32 v129, 4, v255\n"
    "v_mul_u32_u24 v13, 80, v128\n"
    "v_mfma_f32_16x16x32_bf16 v[56:59], v[120:123], v[188:191], v[56:59]\n"
    "v_lshl_add_u32 v13, v129, 3, v13\n"
    "v_add_u32 v13, s56, v13\n"
    "v_lshrrev_b32 v130, 2, v255\n"
    "v_mfma_f32_16x16x32_bf16 v[232:235], v[212:215], v[188:191], v[232:235]\n"
    "buffer_store_dwordx4 v[88:91], v12, s[36:39], s66 offen offset:1024 sc1\n"
    "v_and_b32 v131, 48, v12\n"
    "v_mul_u32_u24 v14, 1792, v130\n"
    "s_waitcnt lgkmcnt(2)\n"
    "v_mfma_f32_16x16x32_bf16 v[84:87], v[76:79], v[48:51], v[84:87]\n"
    "ds_read_b128 v[240:243], v252 offset:18432\n"
    "ds_read_b128 v[200:203], v252 offset:20480\n"
    "ds_read_b128 v[44:47], v252 offset:22528\n"
    "v_mfma_f32_16x16x32_bf16 v[20:23], v[244:247], v[48:51], v[20:23]\n"
    "ds_read_b64_tr_b16 v[100:101], v248 offset:64\n"
    "ds_read_b64_tr_b16 v[102:103], v248 offset:3648\n"
    "ds_read_b64_tr_b16 v[124:125], v248 offset:96\n"
    "v_mfma_f32_16x16x32_bf16 v[176:179], v[8:11], v[48:51], v[176:179]\n"
    "ds_read_b64_tr_b16 v[126:127], v248 offset:3680\n"
    "v_add_u32 v14, v14, v131\n"
    "v_mul_u32_u24 v15, 80, v130\n"
    "s_waitcnt lgkmcnt(7)\n"
    "v_mfma_f32_16x16x32_bf16 v[204:207], v[8:11], v[220:223], v[204:207]\n"
    "v_add3_u32 v15, v15, v131, s56\n"
    "v_lshlrev_b32 v129, 2, v129\n"
    "v_sub_u32 v128, v128, v129\n"
    "v_mfma_f32_16x16x32_bf16 v[56:59], v[244:247], v[220:223], v[56:59]\n"
    "v_mov_b32 v130, 0x3f80\n"
    "v_mov_b32 v131, 0x3f800000\n"
    "v_cmp_eq_u32 s[44:45], 0, v128\n"
    "v_mfma_f32_16x16x32_bf16 v[232:235], v[76:79], v[220:223], v[232:235]\n"
    "v_cmp_eq_u32 s[46:47], 1, v128\n"
    "v_cmp_eq_u32 s[48:49], 2, v128\n"
    "v_cmp_eq_u32 s[50:51], 3, v128\n"
    "s_waitcnt lgkmcnt(2)\n"
    "v_mfma_f32_16x16x32_bf16 v[84:87], v[240:243], v[100:103], v[84:87]\n"
    "ds_read_b128 v[32:35], v253 offset:18432\n"
    "ds_read_b128 v[168:171], v253 offset:20480\n"
    "ds_read_b128 v[24:27], v253 offset:22528\n"
    "v_mfma_f32_16x16x32_bf16 v[20:23], v[200:203], v[100:103], v[20:23]\n"
    "ds_read_b64_tr_b16 v[104:105], v248 offset:7232\n"
    "ds_read_b64_tr_b16 v[106:107], v248 offset:10816\n"
    "ds_read_b64_tr_b16 v[228:229], v248 offset:7264\n"
    "v_mfma_f32_16x16x32_bf16 v[176:179], v[44:47], v[100:103], v[176:179]\n"
    "ds_read_b64_tr_b16 v[230:231], v248 offset:10848\n"
    "v_mov_b32 v117, 0\n"
    "v_mov_b32 v119, 0\n"
    "s_waitcnt lgkmcnt(7)\n"
    "v_mfma_f32_16x16x32_bf16 v[204:207], v[44:47], v[124:127], v[204:207]\n"
    "v_mov_b32 v52, 0\n"
    "v_mov_b32 v54, 0\n"
    "v_mov_b32 v152, 0\n"
    "v_mfma_f32_16x16x32_bf16 v[56:59], v[200:203], v[124:127], v[56:59]\n"
    "v_mov_b32 v154, 0\n"
    "v_cndmask_b32 v53, 0, v130, s[44:45]\n"
    "v_cndmask_b32 v55, 0, v130, s[46:47]\n"
    "v_mfma_f32_16x16x32_bf16 v[232:235], v[240:243], v[124:127], v[232:235]\n"
    "v_cndmask_b32 v153, 0, v130, s[48:49]\n"
    "v_cndmask_b32 v155, 0, v130, s[50:51]\n"
    "v_cndmask_b32 v116, v53, v131, s[46:47]\n"
    "s_waitcnt lgkmcnt(2)\n"
    "v_mfma_f32_16x16x32_bf16 v[84:87], v[32:35], v[104:107], v[84:87]\n"
    "ds_read_b128 v[184:187], v252 offset:0\n"
    "ds_read_b128 v[224:227], v252 offset:2048\n"
    "ds_read_b128 v[196:199], v252 offset:4096\n"
    "v_mfma_f32_16x16x32_bf16 v[20:23], v[168:171], v[104:107], v[20:23]\n"
    "ds_read_b64_tr_b16 v[192:193], v248 offset:16448\n"
    "ds_read_b64_tr_b16 v[194:195], v248 offset:20032\n"
    "ds_read_b64_tr_b16 v[88:89], v248 offset:16480\n"
    "v_mfma_f32_16x16x32_bf16 v[176:179], v[24:27], v[104:107], v[176:179]\n"
    "ds_read_b64_tr_b16 v[90:91], v248 offset:20064\n"
    "v_cndmask_b32 v118, v153, v131, s[50:51]\n"
    "s_waitcnt lgkmcnt(7)\n"
    "v_mfma_f32_16x16x32_bf16 v[204:207], v[24:27], v[228:231], v[204:207]\n"
    "v_mfma_f32_16x16x32_bf16 v[56:59], v[168:171], v[228:231], v[56:59]\n"
    "v_mfma_f32_16x16x32_bf16 v[232:235], v[32:35], v[228:231], v[232:235]\n"
    "s_waitcnt lgkmcnt(2)\n"
    "v_mfma_f32_16x16x32_bf16 v[84:87], v[184:187], v[192:195], v[84:87]\n"
    "ds_read_b128 v[72:75], v253 offset:0\n"
    "ds_read_b128 v[188:191], v253 offset:2048\n"
    "ds_read_b128 v[212:215], v253 offset:4096\n"
    "v_mfma_f32_16x16x32_bf16 v[20:23], v[224:227], v[192:195], v[20:23]\n"
    "ds_read_b64_tr_b16 v[120:121], v248 offset:23616\n"
    "ds_read_b64_tr_b16 v[122:123], v248 offset:27200\n"
    "ds_read_b64_tr_b16 v[0:1], v248 offset:23648\n"
    "v_mfma_f32_16x16x32_bf16 v[176:179], v[196:199], v[192:195], v[176:179]\n"
    "ds_read_b64_tr_b16 v[2:3], v248 offset:27232\n"
    "s_waitcnt lgkmcnt(7)\n"
    "v_mfma_f32_16x16x32_bf16 v[204:207], v[196:199], v[88:91], v[204:207]\n"
    "v_mfma_f32_16x16x32_bf16 v[56:59], v[224:227], v[88:91], v[56:59]\n"
    "v_mfma_f32_16x16x32_bf16 v[232:235], v[184:187], v[88:91], v[232:235]\n"
    "s_waitcnt lgkmcnt(2)\n"
    "v_mfma_f32_16x16x32_bf16 v[84:87], v[72:75], v[120:123], v[84:87]\n"
    "ds_read_b128 v[48:51], v252 offset:6144\n"
    "ds_read_b128 v[220:223], v252 offset:8192\n"
    "ds_read_b128 v[76:79], v252 offset:10240\n"
    "v_mfma_f32_16x16x32_bf16 v[20:23], v[188:191], v[120:123], v[20:23]\n"
    "ds_read_b64_tr_b16 v[244:245], v248 offset:32896\n"
    "ds_read_b64_tr_b16 v[246:247], v248 offset:36480\n"
    "v_mfma_f32_16x16x32_bf16 v[176:179], v[212:215], v[120:123], v[176:179]\n"
    "s_waitcnt lgkmcnt(5)\n"
    "v_mfma_f32_16x16x32_bf16 v[204:207], v[212:215], v[0:3], v[204:207]\n"
    "v_mfma_f32_16x16x32_bf16 v[56:59], v[188:191], v[0:3], v[56:59]\n"
    "v_mfma_f32_16x16x32_bf16 v[232:235], v[72:75], v[0:3], v[232:235]\n"
    "v_cvt_pk_bf16_f32 v84, v84, v85\n"
    "v_cvt_pk_bf16_f32 v85, v86, v87\n"
    "v_cvt_pk_bf16_f32 v86, v20, v21\n"
    "v_cvt_pk_bf16_f32 v87, v22, v23\n"
    "s_waitcnt lgkmcnt(0)\n"
    "v_mfma_f32_16x16x32_bf16 v[8:11], v[48:51], v[244:247], 0\n"
    "ds_read_b128 v[240:243], v253 offset:6144\n"
    "ds_read_b128 v[200:203], v253 offset:8192\n"
    "ds_read_b128 v[44:47], v253 offset:10240\n"
    "v_mfma_f32_16x16x32_bf16 v[100:103], v[220:223], v[244:247], 0\n"
    "ds_read_b64_tr_b16 v[104:105], v248 offset:40064\n"
    "ds_read_b64_tr_b16 v[106:107], v248 offset:43648\n"
    "v_cvt_pk_bf16_f32 v18, v176, v177\n"
    "v_cvt_pk_bf16_f32 v19, v178, v179\n"
    "v_mfma_f32_16x16x32_bf16 v[124:127], v[76:79], v[244:247], 0\n"
    "v_cvt_pk_bf16_f32 v232, v232, v233\n"
    "v_cvt_pk_bf16_f32 v233, v234, v235\n"
    "v_cvt_pk_bf16_f32 v234, v56, v57\n"
    "v_cvt_pk_bf16_f32 v235, v58, v59\n"
    "v_mfma_f32_16x16x32_bf16 v[4:7], v[84:87], v[180:183], v[4:7]\n"
    "ds_read_b128 v[228:231], v252 offset:12288\n"
    "ds_read_b128 v[32:35], v252 offset:14336\n"
    "ds_read_b128 v[168:171], v252 offset:16384\n"
    "v_mfma_f32_16x16x32_bf16 v[236:239], v[84:87], v[216:219], v[236:239]\n"
    "ds_read_b64_tr_b16 v[24:25], v248 offset:49280\n"
    "ds_read_b64_tr_b16 v[26:27], v248 offset:52864\n"
    "v_cvt_pk_bf16_f32 v66, v204, v205\n"
    "v_cvt_pk_bf16_f32 v67, v206, v207\n"
    "v_mfma_f32_16x16x32_bf16 v[132:135], v[84:87], v[80:83], v[132:135]\n"
    "v_mfma_f32_16x16x32_bf16 v[132:135], v[16:19], v[60:63], v[132:135]\n"
    "v_mfma_f32_16x16x32_bf16 v[236:239], v[16:19], v[108:111], v[236:239]\n"
    "v_mfma_f32_16x16x32_bf16 v[4:7], v[16:19], v[96:99], v[4:7]\n"
    "v_mfma_f32_16x16x32_bf16 v[92:95], v[232:235], v[180:183], v[92:95]\n"
    "v_mfma_f32_16x16x32_bf16 v[172:175], v[232:235], v[216:219], v[172:175]\n"
    "v_mfma_f32_16x16x32_bf16 v[164:167], v[232:235], v[80:83], v[164:167]\n"
    "s_nop 2\n"
    "v_cvt_pk_bf16_f32 v132, v132, v133\n"
    "s_nop 0\n"
    "v_cvt_pk_bf16_f32 v133, v4, s78\n"
    "v_cvt_pk_bf16_f32 v134, v134, v135\n"
    "v_mfma_f32_16x16x32_bf16 v[164:167], v[64:67], v[60:63], v[164:167]\n"
    "v_cvt_pk_bf16_f32 v135, v5, s79\n"
    "s_lshl_b32 s66, s72, 13\n"
    "s_add_u32 s66, s66, s60\n"
    "buffer_store_dwordx4 v[132:135], v12, s[36:39], s66 offen offset:0 sc1\n"
    "v_cvt_pk_bf16_f32 v236, v236, v237\n"
    "v_mfma_f32_16x16x32_bf16 v[172:175], v[64:67], v[108:111], v[172:175]\n"
    "v_cvt_pk_bf16_f32 v237, v6, s78\n"
    "v_cvt_pk_bf16_f32 v238, v238, v239\n"
    "v_cvt_pk_bf16_f32 v239, v7, s79\n"
    "v_mfma_f32_16x16x32_bf16 v[92:95], v[64:67], v[96:99], v[92:95]\n"
    "s_waitcnt lgkmcnt(5)\n"
    "v_mfma_f32_16x16x32_bf16 v[124:127], v[44:47], v[104:107], v[124:127]\n"
    "v_mfma_f32_16x16x32_bf16 v[100:103], v[200:203], v[104:107], v[100:103]\n"
    "v_mfma_f32_16x16x32_bf16 v[8:11], v[240:243], v[104:107], v[8:11]\n"
    "buffer_store_dwordx4 v[236:239], v12, s[36:39], s66 offen offset:1024 sc1\n"
    "v_cvt_pk_bf16_f32 v164, v164, v165\n"
    "s_nop 1\n"
    "v_cvt_pk_bf16_f32 v165, v92, s78\n"
    "s_waitcnt lgkmcnt(0)\n"
    "v_mfma_f32_16x16x32_bf16 v[8:11], v[228:231], v[24:27], v[8:11]\n"
    "ds_read_b128 v[192:195], v253 offset:12288\n"
    "ds_read_b128 v[88:91], v253 offset:14336\n"
    "ds_read_b128 v[184:187], v253 offset:16384\n"
    "v_mfma_f32_16x16x32_bf16 v[100:103], v[32:35], v[24:27], v[100:103]\n"
    "ds_read_b64_tr_b16 v[224:225], v248 offset:56448\n"
    "ds_read_b64_tr_b16 v[226:227], v248 offset:60032\n"
    "v_cvt_pk_bf16_f32 v166, v166, v167\n"
    "v_mfma_f32_16x16x32_bf16 v[124:127], v[168:171], v[24:27], v[124:127]\n"
    "v_cvt_pk_bf16_f32 v167, v93, s79\n"
    "s_waitcnt lgkmcnt(0)\n"
    "v_mfma_f32_16x16x32_bf16 v[124:127], v[184:187], v[224:227], v[124:127]\n"
    "ds_read_b128 v[196:199], v252 offset:18432\n"
    "ds_read_b128 v[120:123], v252 offset:20480\n"
    "ds_read_b128 v[20:23], v252 offset:22528\n"
    "v_mfma_f32_16x16x32_bf16 v[100:103], v[88:91], v[224:227], v[100:103]\n"
    "ds_read_b64_tr_b16 v[0:1], v248 offset:128\n"
    "ds_read_b64_tr_b16 v[2:3], v248 offset:3712\n"
    "v_mfma_f32_16x16x32_bf16 v[8:11], v[192:195], v[224:227], v[8:11]\n"
    "s_lshl_b32 s66, s73, 13\n"
    "s_add_u32 s66, s66, s60\n"
    "buffer_store_dwordx4 v[164:167], v12, s[36:39], s66 offen offset:0 sc1\n"
    "v_cvt_pk_bf16_f32 v172, v172, v173\n"
    "v_cvt_pk_bf16_f32 v173, v94, s78\n"
    "s_waitcnt lgkmcnt(0)\n"
    "v_mfma_f32_16x16x32_bf16 v[8:11], v[196:199], v[0:3], v[8:11]\n"
    "ds_read_b128 v[72:75], v253 offset:18432\n"
    "ds_read_b128 v[188:191], v253 offset:20480\n"
    "ds_read_b128 v[212:215], v253 offset:22528\n"
    "v_mfma_f32_16x16x32_bf16 v[100:103], v[120:123], v[0:3], v[100:103]\n"
    "ds_read_b64_tr_b16 v[176:177], v248 offset:7296\n"
    "ds_read_b64_tr_b16 v[178:179], v248 offset:10880\n"
    "v_cvt_pk_bf16_f32 v174, v174, v175\n"
    "v_mfma_f32_16x16x32_bf16 v[124:127], v[20:23], v[0:3], v[124:127]\n"
    "v_cvt_pk_bf16_f32 v175, v95, s79\n"
    "s_waitcnt lgkmcnt(0)\n"
    "v_mfma_f32_16x16x32_bf16 v[124:127], v[212:215], v[176:179], v[124:127]\n"
    "ds_read_b128 v[56:59], v252 offset:0\n"
    "ds_read_b128 v[244:247], v252 offset:2048\n"
    "ds_read_b128 v[48:51], v252 offset:4096\n"
    "v_mfma_f32_16x16x32_bf16 v[100:103], v[188:191], v[176:179], v[100:103]\n"
    "ds_read_b64_tr_b16 v[220:221], v248 offset:16512\n"
    "ds_read_b64_tr_b16 v[222:223], v248 offset:20096\n"
    "v_mfma_f32_16x16x32_bf16 v[8:11], v[72:75], v[176:179], v[8:11]\n"
    "buffer_store_dwordx4 v[172:175], v12, s[36:39], s66 offen offset:1024 sc1\n"
    "s_waitcnt lgkmcnt(0)\n"
    "v_mfma_f32_16x16x32_bf16 v[8:11], v[56:59], v[220:223], v[8:11]\n"
    "ds_read_b128 v[76:79], v253 offset:0\n"
    "ds_read_b128 v[204:207], v253 offset:2048\n"
    "ds_read_b128 v[84:87], v253 offset:4096\n"
    "v_mfma_f32_16x16x32_bf16 v[100:103], v[244:247], v[220:223], v[100:103]\n"
    "ds_read_b64_tr_b16 v[16:17], v248 offset:23680\n"
    "ds_read_b64_tr_b16 v[18:19], v248 offset:27264\n"
    "v_mfma_f32_16x16x32_bf16 v[124:127], v[48:51], v[220:223], v[124:127]\n"
    "s_waitcnt lgkmcnt(0)\n"
    "v_mfma_f32_16x16x32_bf16 v[124:127], v[84:87], v[16:19], v[124:127]\n"
    "ds_read_b128 v[232:235], v252 offset:6144\n"
    "ds_read_b128 v[64:67], v252 offset:8192\n"
    "ds_read_b128 v[132:135], v252 offset:10240\n"
    "v_mfma_f32_16x16x32_bf16 v[100:103], v[204:207], v[16:19], v[100:103]\n"
    "ds_read_b64_tr_b16 v[236:237], v248 offset:32928\n"
    "ds_read_b64_tr_b16 v[238:239], v248 offset:36512\n"
    "v_mfma_f32_16x16x32_bf16 v[8:11], v[76:79], v[16:19], v[8:11]\n"
    "s_waitcnt lgkmcnt(0)\n"
    "v_mfma_f32_16x16x32_bf16 v[4:7], v[232:235], v[236:239], 0\n"
    "ds_read_b128 v[200:203], v253 offset:6144\n"
    "ds_read_b128 v[44:47], v253 offset:8192\n"
    "ds_read_b128 v[24:27], v253 offset:10240\n"
    "v_mfma_f32_16x16x32_bf16 v[104:107], v[64:67], v[236:239], 0\n"
    "ds_read_b64_tr_b16 v[228:229], v248 offset:40096\n"
    "ds_read_b64_tr_b16 v[230:231], v248 offset:43680\n"
    "v_mfma_f32_16x16x32_bf16 v[240:243], v[132:135], v[236:239], 0\n"
    "v_cvt_pk_bf16_f32 v8, v8, v9\n"
    "v_cvt_pk_bf16_f32 v9, v10, v11\n"
    "v_cvt_pk_bf16_f32 v10, v100, v101\n"
    "v_cvt_pk_bf16_f32 v11, v102, v103\n"
    "v_cvt_pk_bf16_f32 v114, v124, v125\n"
    "v_cvt_pk_bf16_f32 v115, v126, v127\n"
    "v_mfma_f32_16x16x32_bf16 v[144:147], v[8:11], v[180:183], v[144:147]\n"
    "ds_read_b128 v[32:35], v252 offset:12288\n"
    "ds_read_b128 v[168:171], v252 offset:14336\n"
    "ds_read_b128 v[224:227], v252 offset:16384\n"
    "v_mfma_f32_16x16x32_bf16 v[136:139], v[8:11], v[216:219], v[136:139]\n"
    "ds_read_b64_tr_b16 v[192:193], v248 offset:49312\n"
    "ds_read_b64_tr_b16 v[194:195], v248 offset:52896\n"
    "v_mfma_f32_16x16x32_bf16 v[68:71], v[8:11], v[80:83], v[68:71]\n"
    "v_mfma_f32_16x16x32_bf16 v[68:71], v[112:115], v[60:63], v[68:71]\n"
    "v_mfma_f32_16x16x32_bf16 v[136:139], v[112:115], v[108:111], v[136:139]\n"
    "v_mfma_f32_16x16x32_bf16 v[144:147], v[112:115], v[96:99], v[144:147]\n"
    "s_waitcnt lgkmcnt(5)\n"
    "v_mfma_f32_16x16x32_bf16 v[240:243], v[24:27], v[228:231], v[240:243]\n"
    "v_mfma_f32_16x16x32_bf16 v[104:107], v[44:47], v[228:231], v[104:107]\n"
    "v_mfma_f32_16x16x32_bf16 v[4:7], v[200:203], v[228:231], v[4:7]\n"
    "s_nop 1\n"
    "v_cvt_pk_bf16_f32 v68, v68, v69\n"
    "s_nop 0\n"
    "v_cvt_pk_bf16_f32 v69, v144, s78\n"
    "v_cvt_pk_bf16_f32 v70, v70, v71\n"
    "s_waitcnt lgkmcnt(0)\n"
    "v_mfma_f32_16x16x32_bf16 v[4:7], v[32:35], v[192:195], v[4:7]\n"
    "ds_read_b128 v[88:91], v253 offset:12288\n"
    "ds_read_b128 v[184:187], v253 offset:14336\n"
    "ds_read_b128 v[0:3], v253 offset:16384\n"
    "v_mfma_f32_16x16x32_bf16 v[104:107], v[168:171], v[192:195], v[104:107]\n"
    "ds_read_b64_tr_b16 v[196:197], v248 offset:56480\n"
    "ds_read_b64_tr_b16 v[198:199], v248 offset:60064\n"
    "v_cvt_pk_bf16_f32 v71, v145, s79\n"
    "v_mfma_f32_16x16x32_bf16 v[240:243], v[224:227], v[192:195], v[240:243]\n"
    "s_lshl_b32 s66, s74, 13\n"
    "s_add_u32 s66, s66, s60\n"
    "buffer_store_dwordx4 v[68:71], v12, s[36:39], s66 offen offset:0 sc1\n"
    "v_cvt_pk_bf16_f32 v136, v136, v137\n"
    "v_cvt_pk_bf16_f32 v137, v146, s78\n"
    "s_waitcnt lgkmcnt(0)\n"
    "v_mfma_f32_16x16x32_bf16 v[240:243], v[0:3], v[196:199], v[240:243]\n"
    "ds_read_b128 v[120:123], v252 offset:18432\n"
    "ds_read_b128 v[20:23], v252 offset:20480\n"
    "ds_read_b128 v[164:167], v252 offset:22528\n"
    "v_mfma_f32_16x16x32_bf16 v[104:107], v[184:187], v[196:199], v[104:107]\n"
    "ds_read_b64_tr_b16 v[172:173], v248 offset:160\n"
    "ds_read_b64_tr_b16 v[174:175], v248 offset:3744\n"
    "v_cvt_pk_bf16_f32 v138, v138, v139\n"
    "v_mfma_f32_16x16x32_bf16 v[4:7], v[88:91], v[196:199], v[4:7]\n"
    "v_cvt_pk_bf16_f32 v139, v147, s79\n"
    "buffer_store_dwordx4 v[136:139], v12, s[36:39], s66 offen offset:1024 sc1\n"
    "s_waitcnt lgkmcnt(0)\n"
    "v_mfma_f32_16x16x32_bf16 v[4:7], v[120:123], v[172:175], v[4:7]\n"
    "ds_read_b128 v[92:95], v253 offset:18432\n"
    "ds_read_b128 v[176:179], v253 offset:20480\n"
    "ds_read_b128 v[72:75], v253 offset:22528\n"
    "v_mfma_f32_16x16x32_bf16 v[104:107], v[20:23], v[172:175], v[104:107]\n"
    "ds_read_b64_tr_b16 v[188:189], v248 offset:7328\n"
    "ds_read_b64_tr_b16 v[190:191], v248 offset:10912\n"
    "v_mfma_f32_16x16x32_bf16 v[240:243], v[164:167], v[172:175], v[240:243]\n"
    "s_waitcnt lgkmcnt(0)\n"
    "v_mfma_f32_16x16x32_bf16 v[240:243], v[72:75], v[188:191], v[240:243]\n"
    "ds_read_b128 v[212:215], v252 offset:0\n"
    "ds_read_b128 v[220:223], v252 offset:2048\n"
    "ds_read_b128 v[56:59], v252 offset:4096\n"
    "v_mfma_f32_16x16x32_bf16 v[104:107], v[176:179], v[188:191], v[104:107]\n"
    "ds_read_b64_tr_b16 v[244:245], v248 offset:16544\n"
    "ds_read_b64_tr_b16 v[246:247], v248 offset:20128\n"
    "v_mfma_f32_16x16x32_bf16 v[4:7], v[92:95], v[188:191], v[4:7]\n"
    "s_waitcnt lgkmcnt(0)\n"
    "v_mfma_f32_16x16x32_bf16 v[4:7], v[212:215], v[244:247], v[4:7]\n"
    "ds_read_b128 v[48:51], v253 offset:0\n"
    "ds_read_b128 v[16:19], v253 offset:2048\n"
    "ds_read_b128 v[76:79], v253 offset:4096\n"
    "v_mfma_f32_16x16x32_bf16 v[104:107], v[220:223], v[244:247], v[104:107]\n"
    "ds_read_b64_tr_b16 v[204:205], v248 offset:23712\n"
    "ds_read_b64_tr_b16 v[206:207], v248 offset:27296\n"
    "v_mfma_f32_16x16x32_bf16 v[240:243], v[56:59], v[244:247], v[240:243]\n"
    "s_waitcnt lgkmcnt(0)\n"
    "v_mfma_f32_16x16x32_bf16 v[240:243], v[76:79], v[204:207], v[240:243]\n"
    "ds_read_b128 v[84:87], v252 offset:6144\n"
    "ds_read_b128 v[100:103], v252 offset:8192\n"
    "ds_read_b128 v[236:239], v252 offset:10240\n"
    "v_mfma_f32_16x16x32_bf16 v[104:107], v[16:19], v[204:207], v[104:107]\n"
    "ds_read_b64_tr_b16 v[232:233], v248 offset:32960\n"
    "ds_read_b64_tr_b16 v[234:235], v248 offset:36544\n"
    "v_mfma_f32_16x16x32_bf16 v[4:7], v[48:51], v[204:207], v[4:7]\n"
    "s_waitcnt lgkmcnt(0)\n"
    "v_mfma_f32_16x16x32_bf16 v[64:67], v[84:87], v[232:235], 0\n"
    "ds_read_b128 v[8:11], v253 offset:6144\n"
    "ds_read_b128 v[112:115], v253 offset:8192\n"
    "ds_read_b128 v[228:231], v253 offset:10240\n"
    "v_mfma_f32_16x16x32_bf16 v[132:135], v[100:103], v[232:235], 0\n"
    "ds_read_b64_tr_b16 v[200:201], v248 offset:40128\n"
    "ds_read_b64_tr_b16 v[202:203], v248 offset:43712\n"
    "v_mfma_f32_16x16x32_bf16 v[124:127], v[236:239], v[232:235], 0\n"
    "v_cvt_pk_bf16_f32 v4, v4, v5\n"
    "v_cvt_pk_bf16_f32 v5, v6, v7\n"
    "v_cvt_pk_bf16_f32 v6, v104, v105\n"
    "v_cvt_pk_bf16_f32 v7, v106, v107\n"
    "v_cvt_pk_bf16_f32 v162, v240, v241\n"
    "v_cvt_pk_bf16_f32 v163, v242, v243\n"
    "v_mfma_f32_16x16x32_bf16 v[156:159], v[4:7], v[180:183], v[156:159]\n"
    "ds_read_b128 v[44:47], v252 offset:12288\n"
    "ds_read_b128 v[24:27], v252 offset:14336\n"
    "ds_read_b128 v[192:195], v252 offset:16384\n"
    "v_mfma_f32_16x16x32_bf16 v[140:143], v[4:7], v[216:219], v[140:143]\n"
    "ds_read_b64_tr_b16 v[32:33], v248 offset:49344\n"
    "ds_read_b64_tr_b16 v[34:35], v248 offset:52928\n"
    "v_mfma_f32_16x16x32_bf16 v[208:211], v[4:7], v[80:83], v[208:211]\n"
    "v_mfma_f32_16x16x32_bf16 v[208:211], v[160:163], v[60:63], v[208:211]\n"
    "v_mfma_f32_16x16x32_bf16 v[140:143], v[160:163], v[108:111], v[140:143]\n"
    "v_mfma_f32_16x16x32_bf16 v[156:159], v[160:163], v[96:99], v[156:159]\n"
    "s_waitcnt lgkmcnt(5)\n"
    "v_mfma_f32_16x16x32_bf16 v[124:127], v[228:231], v[200:203], v[124:127]\n"
    "v_mfma_f32_16x16x32_bf16 v[132:135], v[112:115], v[200:203], v[132:135]\n"
    "v_mfma_f32_16x16x32_bf16 v[64:67], v[8:11], v[200:203], v[64:67]\n"
    "s_waitcnt lgkmcnt(0)\n"
    "v_mfma_f32_16x16x32_bf16 v[64:67], v[44:47], v[32:35], v[64:67]\n"
    "ds_read_b128 v[168:171], v253 offset:12288\n"
    "ds_read_b128 v[224:227], v253 offset:14336\n"
    "ds_read_b128 v[68:71], v253 offset:16384\n"
    "v_mfma_f32_16x16x32_bf16 v[132:135], v[24:27], v[32:35], v[132:135]\n"
    "ds_read_b64_tr_b16 v[136:137], v248 offset:56512\n"
    "ds_read_b64_tr_b16 v[138:139], v248 offset:60096\n"
    "v_mfma_f32_16x16x32_bf16 v[124:127], v[192:195], v[32:35], v[124:127]\n"
    "s_cmp_lg_u32 s28, 3\n"
    "s_cbranch_scc1 L_nosend5\n"
    "v_cvt_pk_bf16_f32 v208, v208, v209\n"
    "v_cvt_pk_bf16_f32 v209, v156, s78\n"
    "v_cvt_pk_bf16_f32 v210, v210, v211\n"
    "v_cvt_pk_bf16_f32 v211, v157, s79\n"
    "s_lshl_b32 s66, s75, 13\n"
    "s_add_u32 s66, s66, s60\n"
    "buffer_store_dwordx4 v[208:211], v12, s[36:39], s66 offen offset:0 sc1\n"
    "v_cvt_pk_bf16_f32 v140, v140, v141\n"
    "v_cvt_pk_bf16_f32 v141, v158, s78\n"
    "v_cvt_pk_bf16_f32 v142, v142, v143\n"
    "v_cvt_pk_bf16_f32 v143, v159, s79\n"
    "buffer_store_dwordx4 v[140:143], v12, s[36:39], s66 offen offset:1024 sc1\n"
    "L_nosend5:\n"
    "s_nop 0\n"
    "s_waitcnt lgkmcnt(0)\n"
    "v_mfma_f32_16x16x32_bf16 v[124:127], v[68:71], v[136:139], v[124:127]\n"
    "ds_read_b128 v[144:147], v252 offset:18432\n"
    "ds_read_b128 v[196:199], v252 offset:20480\n"
    "ds_read_b128 v[88:91], v252 offset:22528\n"
    "v_mfma_f32_16x16x32_bf16 v[132:135], v[224:227], v[136:139], v[132:135]\n"
    "ds_read_b64_tr_b16 v[184:185], v248 offset:192\n"
    "ds_read_b64_tr_b16 v[186:187], v248 offset:3776\n"
    "v_mfma_f32_16x16x32_bf16 v[64:67], v[168:171], v[136:139], v[64:67]\n"
    "s_waitcnt lgkmcnt(0)\n"
    "v_mfma_f32_16x16x32_bf16 v[64:67], v[144:147], v[184:187], v[64:67]\n"
    "ds_read_b128 v[0:3], v253 offset:18432\n"
    "ds_read_b128 v[172:175], v253 offset:20480\n"
    "ds_read_b128 v[120:123], v253 offset:22528\n"
    "v_mfma_f32_16x16x32_bf16 v[132:135], v[196:199], v[184:187], v[132:135]\n"
    "ds_read_b64_tr_b16 v[20:21], v248 offset:7360\n"
    "ds_read_b64_tr_b16 v[22:23], v248 offset:10944\n"
    "v_mfma_f32_16x16x32_bf16 v[124:127], v[88:91], v[184:187], v[124:127]\n"
    "s_waitcnt lgkmcnt(0)\n"
    "v_mfma_f32_16x16x32_bf16 v[124:127], v[120:123], v[20:23], v[124:127]\n"
    "ds_read_b128 v[164:167], v252 offset:0\n"
    "ds_read_b128 v[188:191], v252 offset:2048\n"
    "ds_read_b128 v[92:95], v252 offset:4096\n"
    "v_mfma_f32_16x16x32_bf16 v[132:135], v[172:175], v[20:23], v[132:135]\n"
    "ds_read_b64_tr_b16 v[176:177], v248 offset:16576\n"
    "ds_read_b64_tr_b16 v[178:179], v248 offset:20160\n"
    "v_mfma_f32_16x16x32_bf16 v[64:67], v[0:3], v[20:23], v[64:67]\n"
    "s_waitcnt lgkmcnt(0)\n"
    "v_mfma_f32_16x16x32_bf16 v[64:67], v[164:167], v[176:179], v[64:67]\n"
    "ds_read_b128 v[72:75], v253 offset:0\n"
    "ds_read_b128 v[244:247], v253 offset:2048\n"
    "ds_read_b128 v[212:215], v253 offset:4096\n"
    "v_mfma_f32_16x16x32_bf16 v[132:135], v[188:191], v[176:179], v[132:135]\n"
    "ds_read_b64_tr_b16 v[220:221], v248 offset:23744\n"
    "ds_read_b64_tr_b16 v[222:223], v248 offset:27328\n"
    "v_mfma_f32_16x16x32_bf16 v[124:127], v[92:95], v[176:179], v[124:127]\n"
    "s_waitcnt lgkmcnt(0)\n"
    "v_mfma_f32_16x16x32_bf16 v[124:127], v[212:215], v[220:223], v[124:127]\n"
    "v_mfma_f32_16x16x32_bf16 v[132:135], v[244:247], v[220:223], v[132:135]\n"
    "v_mfma_f32_16x16x32_bf16 v[64:67], v[72:75], v[220:223], v[64:67]\n"
    "s_nop 7\n"
    "v_cvt_pk_bf16_f32 v64, v64, v65\n"
    "v_cvt_pk_bf16_f32 v65, v66, v67\n"
    "v_cvt_pk_bf16_f32 v66, v132, v133\n"
    "v_cvt_pk_bf16_f32 v67, v134, v135\n"
    "v_cvt_pk_bf16_f32 v38, v124, v125\n"
    "v_cvt_pk_bf16_f32 v39, v126, v127\n"
    "v_mfma_f32_16x16x32_bf16 v[148:151], v[64:67], v[80:83], v[148:151]\n"
    "v_mfma_f32_16x16x32_bf16 v[40:43], v[64:67], v[216:219], v[40:43]\n"
    "v_mfma_f32_16x16x32_bf16 v[28:31], v[64:67], v[180:183], v[28:31]\n"
    "v_mfma_f32_16x16x32_bf16 v[28:31], v[36:39], v[96:99], v[28:31]\n"
    "v_mfma_f32_16x16x32_bf16 v[40:43], v[36:39], v[108:111], v[40:43]\n"
    "v_mfma_f32_16x16x32_bf16 v[148:151], v[36:39], v[60:63], v[148:151]\n"
    "s_cmp_eq_u32 s28, 3\n"
    "s_cselect_b32 s63, s76, s75\n"
    "s_add_u32 s68, s28, 3\n"
    "s_and_b32 s68, s68, 3\n"
    "s_lshl_b32 s68, s68, 11\n"
    "s_add_u32 s68, s68, s62\n"
    "s_lshl_b32 s64, s63, 13\n"
    "s_add_u32 s44, s68, s64\n"
    "s_lshl_b32 s64, s76, 13\n"
    "s_add_u32 s45, s68, s64\n"
    "buffer_load_dwordx4 v[56:59], v12, s[36:39], s44 offen offset:0 sc1\n"
    "buffer_load_dwordx4 v[204:207], v12, s[36:39], s44 offen offset:1024 sc1\n"
    "buffer_load_dwordx4 v[48:51], v12, s[36:39], s45 offen offset:0 sc1\n"
    "buffer_load_dwordx4 v[16:19], v12, s[36:39], s45 offen offset:1024 sc1\n"
    "s_cmp_eq_u32 s28, 3\n"
    "s_cselect_b32 s63, s76, s75\n"
    "s_add_u32 s68, s28, 2\n"
    "s_and_b32 s68, s68, 3\n"
    "s_lshl_b32 s68, s68, 11\n"
    "s_add_u32 s68, s68, s62\n"
    "s_lshl_b32 s64, s63, 13\n"
    "s_add_u32 s46, s68, s64\n"
    "s_lshl_b32 s64, s76, 13\n"
    "s_add_u32 s47, s68, s64\n"
    "buffer_load_dwordx4 v[76:79], v12, s[36:39], s46 offen offset:0 sc1\n"
    "buffer_load_dwordx4 v[104:107], v12, s[36:39], s46 offen offset:1024 sc1\n"
    "buffer_load_dwordx4 v[232:235], v12, s[36:39], s47 offen offset:0 sc1\n"
    "buffer_load_dwordx4 v[84:87], v12, s[36:39], s47 offen offset:1024 sc1\n"
    "s_cmp_eq_u32 s28, 3\n"
    "s_cselect_b32 s63, s76, s75\n"
    "s_add_u32 s68, s28, 1\n"
    "s_and_b32 s68, s68, 3\n"
    "s_lshl_b32 s68, s68, 11\n"
    "s_add_u32 s68, s68, s62\n"
    "s_lshl_b32 s64, s63, 13\n"
    "s_add_u32 s48, s68, s64\n"
    "s_lshl_b32 s64, s76, 13\n"
    "s_add_u32 s49, s68, s64\n"
    "buffer_load_dwordx4 v[100:103], v12, s[36:39], s48 offen offset:0 sc1\n"
    "buffer_load_dwordx4 v[236:239], v12, s[36:39], s48 offen offset:1024 sc1\n"
    "buffer_load_dwordx4 v[240:243], v12, s[36:39], s49 offen offset:0 sc1\n"
    "buffer_load_dwordx4 v[4:7], v12, s[36:39], s49 offen offset:1024 sc1\n"
    "s_waitcnt vmcnt(11)\n"
    "v_xor_b32 v128, s78, v57\n"
    "v_xor_b32 v129, s79, v59\n"
    "s_waitcnt vmcnt(10)\n"
    "v_bitop3_b32 v128, v128, v205, s78 bitop3:0xf6\n"
    "v_bitop3_b32 v129, v129, v207, s79 bitop3:0xf6\n"
    "s_waitcnt vmcnt(9)\n"
    "v_bitop3_b32 v128, v128, v49, s78 bitop3:0xf6\n"
    "v_bitop3_b32 v129, v129, v51, s79 bitop3:0xf6\n"
    "s_waitcnt vmcnt(8)\n"
    "v_bitop3_b32 v128, v128, v17, s78 bitop3:0xf6\n"
    "v_bitop3_b32 v129, v129, v19, s79 bitop3:0xf6\n"
    "v_or_b32 v128, v128, v129\n"
    "v_cmp_lt_u32 vcc, 0xffff, v128\n"
    "s_cbranch_vccnz L_xs3\n"
    "L_xb3:\n"
    "v_mfma_f32_16x16x32_bf16 v[148:151], v[116:119], v[48:51], v[148:151]\n"
    "v_mfma_f32_16x16x32_bf16 v[28:31], v[52:55], v[48:51], v[28:31]\n"
    "v_mfma_f32_16x16x32_bf16 v[40:43], v[116:119], v[16:19], v[40:43]\n"
    "v_mfma_f32_16x16x32_bf16 v[28:31], v[152:155], v[16:19], v[28:31]\n"
    "v_mfma_f32_16x16x32_bf16 v[208:211], v[116:119], v[56:59], v[208:211]\n"
    "v_mfma_f32_16x16x32_bf16 v[156:159], v[52:55], v[56:59], v[156:159]\n"
    "v_mfma_f32_16x16x32_bf16 v[140:143], v[116:119], v[204:207], v[140:143]\n"
    "v_mfma_f32_16x16x32_bf16 v[156:159], v[152:155], v[204:207], v[156:159]\n"
    "s_waitcnt vmcnt(7)\n"
    "v_xor_b32 v128, s78, v77\n"
    "v_xor_b32 v129, s79, v79\n"
    "s_waitcnt vmcnt(6)\n"
    "v_bitop3_b32 v128, v128, v105, s78 bitop3:0xf6\n"
    "v_bitop3_b32 v129, v129, v107, s79 bitop3:0xf6\n"
    "s_waitcnt vmcnt(5)\n"
    "v_bitop3_b32 v128, v128, v233, s78 bitop3:0xf6\n"
    "v_bitop3_b32 v129, v129, v235, s79 bitop3:0xf6\n"
    "s_waitcnt vmcnt(4)\n"
    "v_bitop3_b32 v128, v128, v85, s78 bitop3:0xf6\n"
    "v_bitop3_b32 v129, v129, v87, s79 bitop3:0xf6\n"
    "v_or_b32 v128, v128, v129\n"
    "v_cmp_lt_u32 vcc, 0xffff, v128\n"
    "s_cbranch_vccnz L_xs2\n"
    "L_xb2:\n"
    "v_mfma_f32_16x16x32_bf16 v[148:151], v[116:119], v[232:235], v[148:151]\n"
    "v_mfma_f32_16x16x32_bf16 v[28:31], v[52:55], v[232:235], v[28:31]\n"
    "v_mfma_f32_16x16x32_bf16 v[40:43], v[116:119], v[84:87], v[40:43]\n"
    "v_mfma_f32_16x16x32_bf16 v[28:31], v[152:155], v[84:87], v[28:31]\n"
    "v_mfma_f32_16x16x32_bf16 v[208:211], v[116:119], v[76:79], v[208:211]\n"
    "v_mfma_f32_16x16x32_bf16 v[156:159], v[52:55], v[76:79], v[156:159]\n"
    "v_mfma_f32_16x16x32_bf16 v[140:143], v[116:119], v[104:107], v[140:143]\n"
    "v_mfma_f32_16x16x32_bf16 v[156:159], v[152:155], v[104:107], v[156:159]\n"
    "s_waitcnt vmcnt(3)\n"
    "v_xor_b32 v128, s78, v101\n"
    "v_xor_b32 v129, s79, v103\n"
    "s_waitcnt vmcnt(2)\n"
    "v_bitop3_b32 v128, v128, v237, s78 bitop3:0xf6\n"
    "v_bitop3_b32 v129, v129, v239, s79 bitop3:0xf6\n"
    "s_waitcnt vmcnt(1)\n"
    "v_bitop3_b32 v128, v128, v241, s78 bitop3:0xf6\n"
    "v_bitop3_b32 v129, v129, v243, s79 bitop3:0xf6\n"
    "s_waitcnt vmcnt(0)\n"
    "v_bitop3_b32 v128, v128, v5, s78 bitop3:0xf6\n"
    "v_bitop3_b32 v129, v129, v7, s79 bitop3:0xf6\n"
    "v_or_b32 v128, v128, v129\n"
    "v_cmp_lt_u32 vcc, 0xffff, v128\n"
    "s_cbranch_vccnz L_xs1\n"
    "L_xb1:\n"
    "v_mfma_f32_16x16x32_bf16 v[148:151], v[116:119], v[240:243], v[148:151]\n"
    "v_mfma_f32_16x16x32_bf16 v[28:31], v[52:55], v[240:243], v[28:31]\n"
    "v_mfma_f32_16x16x32_bf16 v[40:43], v[116:119], v[4:7], v[40:43]\n"
    "v_mfma_f32_16x16x32_bf16 v[28:31], v[152:155], v[4:7], v[28:31]\n"
    "v_mfma_f32_16x16x32_bf16 v[208:211], v[116:119], v[100:103], v[208:211]\n"
    "v_mfma_f32_16x16x32_bf16 v[156:159], v[52:55], v[100:103], v[156:159]\n"
    "v_mfma_f32_16x16x32_bf16 v[140:143], v[116:119], v[236:239], v[140:143]\n"
    "v_mfma_f32_16x16x32_bf16 v[156:159], v[152:155], v[236:239], v[156:159]\n"
    "s_cmp_eq_u32 s28, 3\n"
    "s_cbranch_scc1 L_skip5\n"
    "v_cvt_pk_bf16_f32 v148, v148, v149\n"
    "v_cvt_pk_bf16_f32 v149, v150, v151\n"
    "ds_write_b64 v13, v[148:149] offset:32\n"
    "v_cvt_pk_bf16_f32 v40, v40, v41\n"
    "v_cvt_pk_bf16_f32 v41, v42, v43\n"
    "ds_write_b64 v13, v[40:41] offset:1312\n"
    "v_cvt_pk_bf16_f32 v28, v28, v29\n"
    "v_cvt_pk_bf16_f32 v29, v30, v31\n"
    "ds_write_b64 v13, v[28:29] offset:2592\n"
    "v_cvt_pk_bf16_f32 v208, v208, v209\n"
    "v_cvt_pk_bf16_f32 v209, v210, v211\n"
    "ds_write_b64 v13, v[208:209] offset:0\n"
    "v_cvt_pk_bf16_f32 v140, v140, v141\n"
    "v_cvt_pk_bf16_f32 v141, v142, v143\n"
    "ds_write_b64 v13, v[140:141] offset:1280\n"
    "v_cvt_pk_bf16_f32 v156, v156, v157\n"
    "v_cvt_pk_bf16_f32 v157, v158, v159\n"
    "ds_write_b64 v13, v[156:157] offset:2560\n"
    "s_lshl_b32 s66, s75, 5\n"
    "ds_read_b128 v[100:103], v15 offset:0\n"
    "ds_read_b128 v[76:79], v15 offset:1280\n"
    "ds_read_b128 v[56:59], v15 offset:2560\n"
    "s_waitcnt lgkmcnt(2)\n"
    "buffer_store_dwordx4 v[100:103], v14, s[40:43], s66 offen sc1\n"
    "s_add_u32 s66, s66, 28672\n"
    "s_waitcnt lgkmcnt(1)\n"
    "buffer_store_dwordx4 v[76:79], v14, s[40:43], s66 offen sc1\n"
    "s_add_u32 s66, s66, 28672\n"
    "s_waitcnt lgkmcnt(0)\n"
    "buffer_store_dwordx4 v[56:59], v14, s[40:43], s66 offen sc1\n"
    "s_branch L_outdone\n"
    "L_skip5:\n"
    "s_nop 7\n"
    "v_cvt_pk_bf16_f32 v148, v148, v149\n"
    "v_cvt_pk_bf16_f32 v149, v150, v151\n"
    "ds_write_b64 v13, v[148:149] offset:0\n"
    "v_cvt_pk_bf16_f32 v40, v40, v41\n"
    "v_cvt_pk_bf16_f32 v41, v42, v43\n"
    "ds_write_b64 v13, v[40:41] offset:1280\n"
    "v_cvt_pk_bf16_f32 v28, v28, v29\n"
    "v_cvt_pk_bf16_f32 v29, v30, v31\n"
    "ds_write_b64 v13, v[28:29] offset:2560\n"
    "s_mov_b32 exec_lo, 0x33333333\n"
    "s_mov_b32 exec_hi, 0x33333333\n"
    "s_lshl_b32 s66, s76, 5\n"
    "ds_read_b128 v[100:103], v15 offset:0\n"
    "ds_read_b128 v[76:79], v15 offset:1280\n"
    "ds_read_b128 v[56:59], v15 offset:2560\n"
    "s_waitcnt lgkmcnt(2)\n"
    "buffer_store_dwordx4 v[100:103], v14, s[40:43], s66 offen sc1\n"
    "s_add_u32 s66, s66, 28672\n"
    "s_waitcnt lgkmcnt(1)\n"
    "buffer_store_dwordx4 v[76:79], v14, s[40:43], s66 offen sc1\n"
    "s_add_u32 s66, s66, 28672\n"
    "s_waitcnt lgkmcnt(0)\n"
    "buffer_store_dwordx4 v[56:59], v14, s[40:43], s66 offen sc1\n"
    "s_mov_b64 exec, -1\n"
    "L_outdone:\n"
    "s_or_b32 s66, s28, s29\n"
    "s_or_b32 s66, s66, %0\n"
    "s_cmp_lg_u32 s66, 0\n"
    "s_cbranch_scc1 L_noep\n"
    "v_mov_b32 v128, s52\n"
    "v_mov_b32 v129, s53\n"
    "s_mov_b64 exec, 1\n"
    "buffer_store_dwordx2 v[128:129], off, s[36:39], s55 sc1\n"
    "s_mov_b64 exec, -1\n"
    "L_noep:\n"
    "s_branch L_end\n"
    "L_xs3:\n"
    "buffer_load_dwordx4 v[56:59], v12, s[36:39], s44 offen offset:0 sc1\n"
    "buffer_load_dwordx4 v[204:207], v12, s[36:39], s44 offen offset:1024 sc1\n"
    "buffer_load_dwordx4 v[48:51], v12, s[36:39], s45 offen offset:0 sc1\n"
    "buffer_load_dwordx4 v[16:19], v12, s[36:39], s45 offen offset:1024 sc1\n"
    "s_sub_u32 s67, s67, 1\n"
    "s_waitcnt vmcnt(0)\n"
    "s_cbranch_scc1 L_xb3\n"
    "v_xor_b32 v128, s78, v57\n"
    "v_xor_b32 v129, s79, v59\n"
    "v_bitop3_b32 v128, v128, v205, s78 bitop3:0xf6\n"
    "v_bitop3_b32 v129, v129, v207, s79 bitop3:0xf6\n"
    "v_bitop3_b32 v128, v128, v49, s78 bitop3:0xf6\n"
    "v_bitop3_b32 v129, v129, v51, s79 bitop3:0xf6\n"
    "v_bitop3_b32 v128, v128, v17, s78 bitop3:0xf6\n"
    "v_bitop3_b32 v129, v129, v19, s79 bitop3:0xf6\n"
    "v_or_b32 v128, v128, v129\n"
    "v_cmp_lt_u32 vcc, 0xffff, v128\n"
    "s_cbranch_vccnz L_xs3\n"
    "s_branch L_xb3\n"
    "L_xs2:\n"
    "buffer_load_dwordx4 v[76:79], v12, s[36:39], s46 offen offset:0 sc1\n"
    "buffer_load_dwordx4 v[104:107], v12, s[36:39], s46 offen offset:1024 sc1\n"
    "buffer_load_dwordx4 v[232:235], v12, s[36:39], s47 offen offset:0 sc1\n"
    "buffer_load_dwordx4 v[84:87], v12, s[36:39], s47 offen offset:1024 sc1\n"
    "s_sub_u32 s67, s67, 1\n"
    "s_waitcnt vmcnt(0)\n"
    "s_cbranch_scc1 L_xb2\n"
    "v_xor_b32 v128, s78, v77\n"
    "v_xor_b32 v129, s79, v79\n"
    "v_bitop3_b32 v128, v128, v105, s78 bitop3:0xf6\n"
    "v_bitop3_b32 v129, v129, v107, s79 bitop3:0xf6\n"
    "v_bitop3_b32 v128, v128, v233, s78 bitop3:0xf6\n"
    "v_bitop3_b32 v129, v129, v235, s79 bitop3:0xf6\n"
    "v_bitop3_b32 v128, v128, v85, s78 bitop3:0xf6\n"
    "v_bitop3_b32 v129, v129, v87, s79 bitop3:0xf6\n"
    "v_or_b32 v128, v128, v129\n"
    "v_cmp_lt_u32 vcc, 0xffff, v128\n"
    "s_cbranch_vccnz L_xs2\n"
    "s_branch L_xb2\n"
    "L_xs1:\n"
    "buffer_load_dwordx4 v[100:103], v12, s[36:39], s48 offen offset:0 sc1\n"
    "buffer_load_dwordx4 v[236:239], v12, s[36:39], s48 offen offset:1024 sc1\n"
    "buffer_load_dwordx4 v[240:243], v12, s[36:39], s49 offen offset:0 sc1\n"
    "buffer_load_dwordx4 v[4:7], v12, s[36:39], s49 offen offset:1024 sc1\n"
    "s_sub_u32 s67, s67, 1\n"
    "s_waitcnt vmcnt(0)\n"
    "s_cbranch_scc1 L_xb1\n"
    "v_xor_b32 v128, s78, v101\n"
    "v_xor_b32 v129, s79, v103\n"
    "v_bitop3_b32 v128, v128, v237, s78 bitop3:0xf6\n"
    "v_bitop3_b32 v129, v129, v239, s79 bitop3:0xf6\n"
    "v_bitop3_b32 v128, v128, v241, s78 bitop3:0xf6\n"
    "v_bitop3_b32 v129, v129, v243, s79 bitop3:0xf6\n"
    "v_bitop3_b32 v128, v128, v5, s78 bitop3:0xf6\n"
    "v_bitop3_b32 v129, v129, v7, s79 bitop3:0xf6\n"
    "v_or_b32 v128, v128, v129\n"
    "v_cmp_lt_u32 vcc, 0xffff, v128\n"
    "s_cbranch_vccnz L_xs1\n"
    "s_branch L_xb1\n"
    "L_end:\n"
    :
    : "s"(wave), "s"((unsigned)blockIdx.x), "s"(__builtin_amdgcn_kernarg_segment_ptr()),
      "s"((unsigned)(size_t)(__attribute__((address_space(3))) char*)smem)
    : "v0", "v1", "v2", "v3", "v4", "v5", "v6", "v7", "v8", "v9", "v10", "v11", "v12", "v13", "v14", "v15", "v16", "v17", "v18", "v19", "v20", "v21", "v22", "v23", "v24", "v25", "v26", "v27", "v28", "v29", "v30", "v31", "v32", "v33", "v34", "v35", "v36", "v37", "v38", "v39", "v40", "v41", "v42", "v43", "v44", "v45", "v46", "v47", "v48", "v49", "v50", "v51", "v52", "v53", "v54", "v55", "v56", "v57", "v58", "v59", "v60", "v61", "v62", "v63", "v64", "v65", "v66", "v67", "v68", "v69", "v70", "v71", "v72", "v73", "v74", "v75", "v76", "v77", "v78", "v79", "v80", "v81", "v82", "v83", "v84", "v85", "v86", "v87", "v88", "v89", "v90", "v91", "v92", "v93", "v94", "v95", "v96", "v97", "v98", "v99", "v100", "v101", "v102", "v103", "v104", "v105", "v106", "v107", "v108", "v109", "v110", "v111", "v112", "v113", "v114", "v115", "v116", "v117", "v118", "v119", "v120", "v121", "v122", "v123", "v124", "v125", "v126", "v127", "v128", "v129", "v130", "v131", "v132", "v133", "v134", "v135", "v136", "v137", "v138", "v139", "v140", "v141", "v142", "v143", "v144", "v145", "v146", "v147", "v148", "v149", "v150", "v151", "v152", "v153", "v154", "v155", "v156", "v157", "v158", "v159", "v160", "v161", "v162", "v163", "v164", "v165", "v166", "v167", "v168", "v169", "v170", "v171", "v172", "v173", "v174", "v175", "v176", "v177", "v178", "v179", "v180", "v181", "v182", "v183", "v184", "v185", "v186", "v187", "v188", "v189", "v190", "v191", "v192", "v193", "v194", "v195", "v196", "v197", "v198", "v199", "v200", "v201", "v202", "v203", "v204", "v205", "v206", "v207", "v208", "v209", "v210", "v211", "v212", "v213", "v214", "v215", "v216", "v217", "v218", "v219", "v220", "v221", "v222", "v223", "v224", "v225", "v226", "v227", "v228", "v229", "v230", "v231", "v232", "v233", "v234", "v235", "v236", "v237", "v238", "v239", "v240", "v241", "v242", "v243", "v244", "v245", "v246", "v247", "v248", "v249", "v250", "v251", "v252", "v253", "v254", "v255", "s9", "s10", "s11", "s12", "s13", "s14", "s15", "s16", "s17", "s18", "s19", "s20", "s21", "s22", "s23", "s24", "s25", "s26", "s27", "s28", "s29", "s30", "s31", "s33", "s34", "s35", "s36", "s37", "s38", "s39", "s40", "s41", "s42", "s43", "s44", "s45", "s46", "s47", "s48", "s49", "s50", "s51", "s52", "s53", "s54", "s55", "s56", "s57", "s58", "s59", "s60", "s61", "s62", "s63", "s64", "s65", "s66", "s67", "s68", "s69", "s70", "s71", "s72", "s73", "s74", "s75", "s76", "s77", "s78", "s79", "s80", "s81", "s82", "s83", "s84", "s85", "s86", "s87", "s88", "s89", "s90", "s91", "s92", "s93", "s94", "s95", "s96", "s97", "s98", "s99", "vcc", "scc", "memory");
}

extern "C" void kernel_launch(void* const* d_in, const int* in_sizes, int n_in, void* d_out, int out_size, void* d_ws,
                              size_t ws_size, hipStream_t stream) {
  fused_attn_pair<<<256, 512, 0, stream>>>((const char*)d_in[0], (const char*)d_in[1], (const char*)d_in[2], (const char*)d_in[3],
                                           (const char*)d_in[4], (const char*)d_in[5], (const char*)d_in[6], (const char*)d_in[7],
                                           (const char*)d_in[8], (const char*)d_in[9], (char*)d_out, (char*)d_ws);
}
```
